```python
import torch

def _softmax(x, axis=-1):
    return torch.softmax(x, dim=axis)
def _gelu(x):
    return torch.nn.functional.gelu(x, approximate="tanh")
def _silu(x):
    return torch.nn.functional.silu(x)
def _sigmoid(x):
    return torch.sigmoid(x)
def _layernorm(x, axis=-1, eps=1e-5):
    m = x.mean(dim=axis, keepdim=True)
    v = x.var(dim=axis, keepdim=True, unbiased=False)
    return (x - m) / torch.sqrt(v + eps)
def _scatter_add(tgt, idx, src, axis=0):
    return tgt.clone().index_add_(axis, idx, src)
def _segment_sum(src, seg, n):
    out = torch.zeros((n,) + src.shape[1:], dtype=src.dtype)
    return out.index_add_(0, seg, src)
def _pad(x, pw):
    flat = tuple(v for pair in reversed(pw) for v in pair)
    return torch.nn.functional.pad(x, flat)

kernel_name = 'sequential_preLN__ssm_conv_s4d__dense_activation_relu__prompt'

def setup_inputs():

    _w = (lambda a: (lambda t: t.to(torch.bfloat16) if t.is_floating_point() else t)(torch.as_tensor(a)))
    def _A_setup():
        Bsz, L, D, N = 1, 2048, 512, 128
        return {
            "u": _w((lambda *s: torch.randn(*s, dtype=torch.bfloat16) * 0.05)(Bsz, L, D)),
            "log_A_real": _w((lambda *s: torch.randn(*s, dtype=torch.bfloat16) * 6.4)(D, N) * 0.5),
            "A_imag": _w((lambda *s: torch.randn(*s, dtype=torch.bfloat16) * 0.05)(D, N) * 3.14),
            "C_real": _w((lambda *s: torch.randn(*s, dtype=torch.bfloat16) * 0.05)(D, N) / N ** 0.5),
            "C_imag": _w((lambda *s: torch.randn(*s, dtype=torch.bfloat16) * 0.05)(D, N) / N ** 0.5),
            "log_dt": _w((lambda *s: torch.randn(*s, dtype=torch.bfloat16) * 0.05)(D) * 0.5 - 4.0),
            "D_skip": _w((lambda *s: torch.randn(*s, dtype=torch.bfloat16) * 0.05)(D)),
        }
    _A_in = _A_setup()

    def _B_setup():
        B, S, D, Dff = 1, 2048, 512, 1792
        return {
            "x": _w((lambda *s: torch.randn(*s, dtype=torch.bfloat16) * 0.05)(B, S, D)),
            "w1": _w((lambda *s: torch.randn(*s, dtype=torch.bfloat16) * 0.05)(D, Dff)),
            "w2": _w((lambda *s: torch.randn(*s, dtype=torch.bfloat16) * 0.05)(Dff, D)),
        }
    _B_in = _B_setup()

    return {
        "A_u": _A_in["u"],
        "A_log_A_real": _A_in["log_A_real"],
        "A_A_imag": _A_in["A_imag"],
        "A_C_real": _A_in["C_real"],
        "A_C_imag": _A_in["C_imag"],
        "A_log_dt": _A_in["log_dt"],
        "A_D_skip": _A_in["D_skip"],
        "B_x": _B_in["x"],
        "B_w1": _B_in["w1"],
        "B_w2": _B_in["w2"],
    }

def reference(A_u, A_log_A_real, A_A_imag, A_C_real, A_C_imag, A_log_dt, A_D_skip, B_x, B_w1, B_w2):
    def _A_ref(u, log_A_real, A_imag, C_real, C_imag, log_dt, D_skip):
        L = u.shape[1]
        dt = torch.exp(log_dt)
        A = -torch.exp(log_A_real) + 1j * A_imag
        C = (C_real + 1j * C_imag) * dt[:, None]
        pos = (lambda x, dt: x.to(dt))(torch.arange(L), log_dt.dtype)
        P = torch.exp((A * dt[:, None])[:, :, None] * pos[None, None, :])
        K = 2.0 * torch.einsum("dn,dnl->dl", C, P).real
        L2 = 2 * L
        u_t = (lambda x, *d: x.permute(*d))(u, 0, 2, 1)
        Kf = (lambda x, *a, **k: torch.fft.rfft(x.float(), *a, **k))(K, L2)
        Uf = (lambda x, *a, **k: torch.fft.rfft(x.float(), *a, **k))(u_t, L2)
        y = (lambda x, *a, **k: torch.fft.irfft(x, *a, **k).to(torch.bfloat16))(Uf * Kf[None, :, :], L2)[:, :, :L]
        y = (lambda x, *d: x.permute(*d))(y, 0, 2, 1)
        return y + u * D_skip[None, None, :]

    def _B_ref(x, w1, w2):
        h = torch.einsum("bsd,df->bsf", x, w1)
        zero = torch.zeros_like(h)
        h = torch.maximum(h, zero)
        return torch.einsum("bsf,fd->bsd", h, w2)

    x = A_u
    h = x + _A_ref(_layernorm(x, axis=-1), A_log_A_real, A_A_imag, A_C_real, A_C_imag, A_log_dt, A_D_skip)
    h2 = h + _B_ref(_layernorm(h, axis=-1), B_w1, B_w2)
    return h2
```

```cpp
#include <hip/hip_runtime.h>
#include <hip/hip_bf16.h>
#include <stdint.h>

typedef unsigned short u16;
static constexpr int L = 2048, D = 512, N = 128, F = 1792;

__device__ __forceinline__ float bf2f(u16 v) { return __uint_as_float(((unsigned)v) << 16); }
__device__ __forceinline__ u16 f2bf(float f) {
  unsigned u = __float_as_uint(f);
  return (u16)((u + 0x7FFF + ((u >> 16) & 1)) >> 16);
}
__device__ __forceinline__ float rbf(float f) { return bf2f(f2bf(f)); }

__global__ void k_ln(const u16* x, u16* u) {
  int row = blockIdx.x; int t = threadIdx.x;
  float v[8]; float s = 0;
  for (int i = 0; i < 8; i++) { v[i] = bf2f(x[row * D + t * 8 + i]); s += v[i]; }
  for (int o = 32; o; o >>= 1) s += __shfl_xor(s, o);
  float m = s / D; float q = 0;
  for (int i = 0; i < 8; i++) { float d = v[i] - m; q += d * d; }
  for (int o = 32; o; o >>= 1) q += __shfl_xor(q, o);
  float mb = rbf(m), vb = rbf(q / D);
  float sd = rbf(sqrtf(rbf(vb + 1e-5f)));
  for (int i = 0; i < 8; i++) u[row * D + t * 8 + i] = f2bf(rbf(v[i] - mb) / sd);
}
__global__ void k_kgen(const u16* lar, const u16* aim, const u16* cr, const u16* ci, const u16* ldt, float* K) {
  int d = blockIdx.y; int l = blockIdx.x * 256 + threadIdx.x;
  float dt = rbf(expf(bf2f(ldt[d])));
  float pos = rbf((float)l);
  float acc = 0;
  for (int n = 0; n < N; n++) {
    float ar = -rbf(expf(bf2f(lar[d * N + n]))) * dt, ai = bf2f(aim[d * N + n]) * dt;
    float c_r = bf2f(cr[d * N + n]) * dt, c_i = bf2f(ci[d * N + n]) * dt;
    float e = expf(ar * pos); float sn, cs; sincosf(ai * pos, &sn, &cs);
    acc += c_r * e * cs - c_i * e * sn;
  }
  K[d * L + l] = 2.f * acc;
}
__global__ void k_conv(const u16* x, const u16* u, const float* K, const u16* dsk, u16* h) {
  int t = blockIdx.x; int d = threadIdx.x;
  float acc = 0;
  for (int s = 0; s <= t; s++) acc += K[d * L + s] * bf2f(u[(t - s) * D + d]);
  float y = rbf(acc);
  float a = rbf(y + rbf(bf2f(u[t * D + d]) * bf2f(dsk[d])));
  h[t * D + d] = f2bf(bf2f(x[t * D + d]) + a);
}
__global__ void k_g1(const u16* a, const u16* w1, u16* h1) {
  int t = blockIdx.y; int f = blockIdx.x * 256 + threadIdx.x;
  float acc = 0;
  for (int k = 0; k < D; k++) acc += bf2f(a[t * D + k]) * bf2f(w1[k * F + f]);
  h1[t * F + f] = f2bf(fmaxf(rbf(acc), 0.f));
}
__global__ void k_g2(const u16* h1, const u16* w2, const u16* h, u16* out) {
  int t = blockIdx.x; int d = threadIdx.x;
  float acc = 0;
  for (int k = 0; k < F; k++) acc += bf2f(h1[t * F + k]) * bf2f(w2[k * D + d]);
  out[t * D + d] = f2bf(bf2f(h[t * D + d]) + rbf(acc));
}

extern "C" void kernel_launch(void* const* d_in, const int* in_sizes, int n_in,
                              void* d_out, int out_size, void* d_ws, size_t ws_size,
                              hipStream_t stream) {
  const u16* x = (const u16*)d_in[0];
  char* ws = (char*)d_ws;
  u16* u = (u16*)ws; float* K = (float*)(ws + (4 << 20)); u16* h = (u16*)(ws + (12 << 20));
  u16* a2 = (u16*)(ws + (16 << 20)); u16* h1 = (u16*)(ws + (20 << 20));
  k_ln<<<L, 64, 0, stream>>>(x, u);
  k_kgen<<<dim3(L / 256, D), 256, 0, stream>>>((const u16*)d_in[1], (const u16*)d_in[2], (const u16*)d_in[3], (const u16*)d_in[4], (const u16*)d_in[5], K);
  k_conv<<<L, D, 0, stream>>>(x, u, K, (const u16*)d_in[6], h);
  k_ln<<<L, 64, 0, stream>>>(h, a2);
  k_g1<<<dim3(F / 256, L), 256, 0, stream>>>(a2, (const u16*)d_in[8], h1);
  k_g2<<<L, D, 0, stream>>>(h1, (const u16*)d_in[9], h, (u16*)d_out);
}
```

```cpp
#include <hip/hip_runtime.h>
#include <stdint.h>

typedef unsigned short u16;
typedef unsigned u32;
typedef u32 u32x2 __attribute__((ext_vector_type(2)));
typedef u32 u32x4 __attribute__((ext_vector_type(4)));
typedef float f32x2 __attribute__((ext_vector_type(2)));
typedef float f32x4 __attribute__((ext_vector_type(4)));
typedef short s16x4 __attribute__((ext_vector_type(4)));
typedef short s16x2 __attribute__((ext_vector_type(2)));
typedef __bf16 bf16x8 __attribute__((ext_vector_type(8)));
typedef __bf16 bf16x2 __attribute__((ext_vector_type(2)));
typedef __amdgpu_buffer_rsrc_t rsrc_t;
#define LDS __attribute__((address_space(3)))
#define DI __device__ __forceinline__

constexpr u32 WS_REC = 4096, WS_FLAG2 = 32768, WS_FLAG3 = 40960;
constexpr u32 WS_XT = 1u << 20, WS_HT = 4u << 20, WS_H1 = 8u << 20, WS_H1S = 16u << 20, WS_STAMP = 32u << 20;
constexpr u32 WS_BYTES = 64u << 20;
constexpr int L_PART = 0, L_TAG = 512, L_SUM = 1024, L_SQ = 1536;
constexpr int L_A = 2048;
constexpr int TABP = 544, TABC = 53 * TABP;
constexpr int L_KC = L_A, KCP = 4160, KCC = 4 * KCP;
constexpr int L_UI = L_A + 2 * KCC, UIC = 4608;
constexpr int L_EX = L_UI + 2 * UIC;
constexpr int L_KPOS = L_A + 58880, KPC = 4112;
constexpr int L_ST = L_KPOS + 2 * KPC + 224;
constexpr int L_S3 = L_KPOS;
constexpr int L_HT = 2048;
constexpr int LDS_BYTES = 163840;
static_assert(L_ST % 256 == 0 && L_ST + 13 * 7168 + 32 <= LDS_BYTES && L_S3 % 256 == 0 && L_S3 + 3 * 7168 + 18 * 4096 <= LDS_BYTES, "");
static_assert(2 * TABC <= 58880 && L_EX + 16384 <= L_KPOS, "");

DI float bf_lo(u32 v) { return __uint_as_float(v << 16); }
DI float bf_hi(u32 v) { return __uint_as_float(v & 0xffff0000u); }
DI u32 pk_bf16(float a, float b) { f32x2 t = {a, b}; return __builtin_bit_cast(u32, __builtin_convertvector(t, bf16x2)); }
DI float rbf(float a) { return bf_lo(pk_bf16(a, a)); }
DI f32x4 mfma16(u32x4 a, u32x4 b, f32x4 c) {
  return __builtin_amdgcn_mfma_f32_16x16x32_bf16(__builtin_bit_cast(bf16x8, a), __builtin_bit_cast(bf16x8, b), c, 0, 0, 0);
}
DI u32x2 tr_read(LDS char* p) { return __builtin_bit_cast(u32x2, __builtin_amdgcn_ds_read_tr16_b64_v4i16((LDS s16x4*)p)); }
DI u32x4 tr_frag(LDS char* p0, LDS char* p1) { u32x2 a = tr_read(p0), b = tr_read(p1); return u32x4{a.x, a.y, b.x, b.y}; }
DI u32x4 ld128(LDS char* p) { return *(LDS u32x4*)p; }
DI rsrc_t mk_rsrc(const void* p, u32 bytes) { return __builtin_amdgcn_make_buffer_rsrc((void*)p, 0, bytes, 0x00020000); }
#define WAITV(n) asm volatile("s_waitcnt vmcnt(" #n ")" ::: "memory")
#define BAR() asm volatile("s_waitcnt lgkmcnt(0)\n\ts_barrier" ::: "memory")
#define SCHED() __builtin_amdgcn_sched_barrier(0)
DI void dma16(u32 m0v, u32 voff, rsrc_t r, u32 soff) {
  asm volatile("s_mov_b32 m0, %0\n\ts_nop 0\n\tbuffer_load_dwordx4 %1, %2, %3 offen lds" ::"s"(m0v), "v"(voff), "s"(r), "s"(soff) : "memory");
}
DI void dma16c(u32 m0v, u32 voff, rsrc_t r, u32 soff) {
  asm volatile("s_mov_b32 m0, %0\n\ts_nop 0\n\tbuffer_load_dwordx4 %1, %2, %3 offen sc1 lds" ::"s"(m0v), "v"(voff), "s"(r), "s"(soff) : "memory");
}
DI f32x2 cmul(f32x2 a, f32x2 b) {
  f32x2 t = f32x2{a.x, a.x} * b;
  return f32x2{__builtin_fmaf(-a.y, b.y, t.x), __builtin_fmaf(a.y, b.x, t.y)};
}
DI f32x2 cexp_k(float ar, float ai, float e) {
  float m = __builtin_amdgcn_exp2f(ar * e), ph = __builtin_amdgcn_fractf(ai * e);
  return f32x2{m * __builtin_amdgcn_cosf(ph), m * __builtin_amdgcn_sinf(ph)};
}
DI f32x4 sel4(bool c, f32x4 a, f32x4 b) { return c ? a : b; }
DI float pick4(f32x4 v, int p) { const float a = v[0], b = v[1], c = v[2], d = v[3]; const float lo = (p & 1) ? b : a, hi = (p & 1) ? d : c; return (p & 2) ? hi : lo; }
DI u32 relu_pk(u32 v) { const s16x2 z = {0, 0}; return __builtin_bit_cast(u32, __builtin_elementwise_max(__builtin_bit_cast(s16x2, v), z)); }
constexpr u32 ONES = 0x3f803f80u;
constexpr int kSpin = 1 << 15;

#define STAMP(i) do { } while (0)

DI constexpr int ks_of(int kh, int i) { return i < 24 ? 24 * kh + i : 48 + 4 * kh + (i - 24); }
DI constexpr int f0_of(int ks) { return ks < 48 ? 112 * (ks / 3) + 32 * (ks % 3) : 224 * (ks - 48) + 96; }

__global__ __launch_bounds__(256, 1) void fused_block(const u16* __restrict__ x, const u16* __restrict__ p_la, const u16* __restrict__ p_ai,
                                                      const u16* __restrict__ p_cr, const u16* __restrict__ p_ci, const u32* __restrict__ p_ldt,
                                                      const u32* __restrict__ p_dsk, const u16* __restrict__ w1, const u16* __restrict__ w2,
                                                      u16* __restrict__ out, char* __restrict__ ws) {
  extern __shared__ __attribute__((aligned(256))) char smem_g[];
  LDS char* sm = (LDS char*)smem_g;
  const u32 lds0 = (u32)(uintptr_t)sm;
  STAMP(0);
  const int b = blockIdx.x;
  const int id = (b & 7) * 32 + (b >> 3);
  const int tid = threadIdx.x, l = tid & 63;
  const int w = __builtin_amdgcn_readfirstlane(tid >> 6);
  const int c16 = l & 15, g4 = l >> 4, q4 = (l >> 2) & 3, p4 = l & 3;
  const rsrc_t rX = mk_rsrc(x, 2u << 20), rWS = mk_rsrc(ws, WS_BYTES);
  const rsrc_t rW1 = mk_rsrc(w1, 512 * 1792 * 2), rW2 = mk_rsrc(w2, 512 * 1792 * 2);
  const int grp = id >> 4, jt = id & 15, half = jt >> 3, ct = jt & 7;
  const int ch = w >> 1, eh = w & 1;

  u32 pla, pai, pcr, pci;
  u32x4 xs0, xs1;
  {
    const u32 vo = ((2 * id + ch) * 128 + eh * 64 + l) * 2;
    const rsrc_t r0 = mk_rsrc(p_la, 131072), r1 = mk_rsrc(p_ai, 131072), r2 = mk_rsrc(p_cr, 131072), r3 = mk_rsrc(p_ci, 131072);
    asm volatile("buffer_load_ushort %0, %8, %4, 0 offen\n\tbuffer_load_ushort %1, %8, %5, 0 offen\n\t"
                 "buffer_load_ushort %2, %8, %6, 0 offen\n\tbuffer_load_ushort %3, %8, %7, 0 offen"
                 : "=&v"(pla), "=&v"(pai), "=&v"(pcr), "=&v"(pci) : "s"(r0), "s"(r1), "s"(r2), "s"(r3), "v"(vo) : "memory");
    const u32 vs = (8 * id + (c16 & 7)) * 1024 + (16 * w + 2 * g4 + (c16 >> 3)) * 16;
    asm volatile("buffer_load_dwordx4 %0, %2, %3, 0 offen\n\tbuffer_load_dwordx4 %1, %2, %3, 0 offen offset:128"
                 : "=&v"(xs0), "=&v"(xs1) : "v"(vs), "s"(rX) : "memory");
  }
  u32 w1off[7];
#pragma unroll
  for (int i = 0; i < 7; i++) { const u32 n = 64 * i + l, row = (n * 4682) >> 16; w1off[i] = row * 3584 + (n - row * 14) * 16 + jt * 224; }
  u32 epoch = 0;
  u32x2 xt[8];
  if (w < 2) {
    const u32 vt = ((id >> 3) * 64 + (l & 7) * 8) * 1024 + (id & 7) * 128 + (w * 8 + (l >> 3)) * 8;
    const u32 k4096 = 4096, zero = 0;
    asm volatile("buffer_load_dword %8, %12, %11, 0 offen sc1\n\t"
                 "buffer_load_dwordx2 %0, %9, %10, 0 offen\n\tbuffer_load_dwordx2 %1, %9, %10, 0 offen offset:1024\n\t"
                 "buffer_load_dwordx2 %2, %9, %10, 0 offen offset:2048\n\tbuffer_load_dwordx2 %3, %9, %10, 0 offen offset:3072\n\t"
                 "buffer_load_dwordx2 %4, %9, %10, %13 offen\n\tbuffer_load_dwordx2 %5, %9, %10, %13 offen offset:1024\n\t"
                 "buffer_load_dwordx2 %6, %9, %10, %13 offen offset:2048\n\tbuffer_load_dwordx2 %7, %9, %10, %13 offen offset:3072"
                 : "=&v"(xt[0]), "=&v"(xt[1]), "=&v"(xt[2]), "=&v"(xt[3]), "=&v"(xt[4]), "=&v"(xt[5]), "=&v"(xt[6]), "=&v"(xt[7]), "=&v"(epoch)
                 : "v"(vt), "s"(rX), "s"(rWS), "v"(zero), "s"(k4096) : "memory");
  } else {
#pragma unroll
    for (int k = 0; k < 3; k++)
#pragma unroll
      for (int i = 0; i < 7; i++) dma16(lds0 + L_ST + (2 * k + w - 2) * 7168 + i * 1024, w1off[i], rW1, (2 * k + w - 2) * 32 * 3584);
  }
  u32 kaddr[11];
#pragma unroll
  for (int e = 0; e < 11; e++) {
    const int pos = 8 * tid - 3 + e;
    const u32 r = (u32)rbf((float)pos);
    kaddr[e] = (pos < 0 ? 2050u : r) * 2;
  }
  const u32 ldt2 = p_ldt[id], dsk2 = p_dsk[id];
  STAMP(1);
  if (w < 2) WAITV(9); else WAITV(21);
  asm volatile("" : "+v"(xs0), "+v"(xs1), "+v"(pla), "+v"(pai), "+v"(pcr), "+v"(pci)::"memory");
  STAMP(2);
  {
    const u32x4 ones = {ONES, ONES, ONES, ONES};
    f32x4 s = {0, 0, 0, 0}, q = {0, 0, 0, 0};
    s = mfma16(xs0, ones, s); q = mfma16(xs0, xs0, q);
    s = mfma16(xs1, ones, s); q = mfma16(xs1, xs1, q);
    const float sv = pick4(s, p4);
    const float qv = pick4(q, p4);
    if (q4 == g4) *(LDS f32x2*)(sm + L_PART + (c16 & 7) * 64 + (2 * w + (c16 >> 3)) * 8) = f32x2{sv, qv};
  }
  if (w < 2) {
    asm volatile("s_waitcnt vmcnt(0)" : "+v"(xt[0]), "+v"(xt[1]), "+v"(xt[2]), "+v"(xt[3]), "+v"(xt[4]), "+v"(xt[5]), "+v"(xt[6]), "+v"(xt[7]), "+v"(epoch)::"memory");
    const u32 vo = WS_XT + ((id & 7) * 64 + (w * 8 + (l >> 3)) * 4) * 4096 + ((id >> 3) * 64 + (l & 7) * 8) * 2;
#pragma unroll
    for (int c = 0; c < 4; c++) {
      u32x4 o;
#pragma unroll
      for (int k = 0; k < 4; k++) o[k] = __builtin_amdgcn_perm(xt[2 * k + 1][c >> 1], xt[2 * k][c >> 1], (c & 1) ? 0x07060302u : 0x05040100u);
      __builtin_amdgcn_raw_buffer_store_b128(o, rWS, vo + c * 4096, 0, 16);
    }
    if (tid == 0) *(LDS u32*)(sm + L_TAG) = epoch + 1;
    if (tid == 1) { *(LDS u32*)(sm + L_KPOS + 4100) = 0; *(LDS u32*)(sm + L_KPOS + KPC + 4100) = 0; }
    WAITV(0);
  }
  BAR();
  STAMP(3);
  const u32 tag = __builtin_amdgcn_readfirstlane(*(LDS u32*)(sm + L_TAG));
  if (w == 0) {
    if (l < 8) {
      f32x4 a0 = *(LDS f32x4*)(sm + L_PART + l * 64), a1 = *(LDS f32x4*)(sm + L_PART + l * 64 + 16);
      f32x4 a2 = *(LDS f32x4*)(sm + L_PART + l * 64 + 32), a3 = *(LDS f32x4*)(sm + L_PART + l * 64 + 48);
      a0 = (a0 + a1) + (a2 + a3);
      const float m = (a0[0] + a0[2]) * (1.f / 512), var = fmaxf((a0[1] + a0[3]) * (1.f / 512) - m * m, 0.f);
      const float sd = __builtin_amdgcn_sqrtf(rbf(rbf(var) + 1e-5f));
      __builtin_amdgcn_raw_buffer_store_b64(u32x2{pk_bf16(m, sd), tag}, rWS, WS_REC + id * 64 + l * 8, 0, 16);
    }
  }
  {
    const int k0 = w == 0 ? 12 : 4 + 2 * w;
#pragma unroll
    for (int i = 0; i < 7; i++) dma16(lds0 + L_ST + k0 * 7168 + i * 1024, w1off[i], rW1, k0 * 32 * 3584);
    if (w != 0) {
#pragma unroll
      for (int i = 0; i < 7; i++) dma16(lds0 + L_ST + (k0 + 1) * 7168 + i * 1024, w1off[i], rW1, (k0 + 1) * 32 * 3584);
    }
  }
  STAMP(4);
  {
    const float dt = rbf(__builtin_amdgcn_exp2f((ch ? bf_hi(ldt2) : bf_lo(ldt2)) * 1.44269504f));
    const float Ar = rbf(__builtin_amdgcn_exp2f(bf_lo(pla) * 1.44269504f));
    const float ar = -(Ar * dt) * 1.44269504f, ai = (bf_lo(pai) * dt) * 0.15915494f;
    LDS char* tf = sm + L_A + ch * TABC + (eh * 64 + l) * 4;
#define PUT(row, v) (*(LDS u32*)(tf + (row) * TABP) = pk_bf16((v).x, (v).y))
    const f32x2 c0 = {2.f * (bf_lo(pcr) * dt), -2.f * (bf_lo(pci) * dt)};
    const f32x2 v8 = cexp_k(ar, -ai, 8.f), z1 = cexp_k(ar, ai, 1.f), z256 = cexp_k(ar, ai, 256.f);
    const f32x2 v16 = cmul(v8, v8), v32 = cmul(v16, v16);
    f32x2 f0 = c0, f1 = cmul(c0, v8), f2 = cmul(c0, v16), f3 = cmul(f1, v16);
#pragma unroll
    for (int s = 0; s < 8; s++) {
      PUT(4 * s, f0); PUT(4 * s + 1, f1); PUT(4 * s + 2, f2); PUT(4 * s + 3, f3);
      if (s < 7) { f0 = cmul(f0, v32); f1 = cmul(f1, v32); f2 = cmul(f2, v32); f3 = cmul(f3, v32); }
    }
    const f32x2 z2 = cmul(z1, z1), z3 = cmul(z2, z1), z4 = cmul(z2, z2), z5 = cmul(z4, z1), z6 = cmul(z3, z3), z7 = cmul(z4, z3);
    const f32x2 z512 = cmul(z256, z256), z768 = cmul(z512, z256), z1024 = cmul(z512, z512);
    const f32x2 one = {1.f, 0.f};
    PUT(32, one); PUT(33, z1); PUT(34, z2); PUT(35, z3); PUT(36, z4); PUT(37, z5); PUT(38, z6); PUT(39, z7);
    PUT(40, z256); PUT(41, cmul(z256, z2)); PUT(42, cmul(z256, z4)); PUT(43, cmul(z256, z6));
    PUT(44, z512); PUT(45, cmul(z512, z4)); PUT(46, z768); PUT(47, cmul(z768, z4));
    PUT(48, z1024); PUT(49, cmul(z1024, z256)); PUT(50, cmul(z1024, z512)); PUT(51, cmul(z1024, z768)); PUT(52, cmul(z1024, z1024));
#undef PUT
    BAR();
    LDS char* tb = sm + L_A + ch * TABC + g4 * 16;
    LDS char* pf = tb + (16 * eh + c16) * TABP;
    LDS char* pa = tb + (32 + c16) * TABP;
    LDS char* pb = tb + (48 + (c16 < 4 ? c16 : 4)) * TABP;
    f32x4 k0 = {0, 0, 0, 0}, k1 = {0, 0, 0, 0};
#pragma unroll
    for (int ks = 0; ks < 8; ks++) {
      const u32x4 fb = ld128(pf + 64 * ks);
      k0 = mfma16(ld128(pa + 64 * ks), fb, k0);
      k1 = mfma16(ld128(pb + 64 * ks), fb, k1);
    }
    if (eh == 0 && l == 0) k0[0] += ch ? bf_hi(dsk2) : bf_lo(dsk2);
    LDS char* kp = sm + L_KPOS + ch * KPC + (16 * eh + c16) * 16;
    const int s0 = g4 < 2 ? 1 : g4 == 2 ? 2 : 4, b0 = g4 < 2 ? 4 * g4 : 256 * (g4 - 1);
    const u32 a01 = pk_bf16(k0[0], k0[1]), a23 = pk_bf16(k0[2], k0[3]), c01 = pk_bf16(k1[0], k1[1]), c23 = pk_bf16(k1[2], k1[3]);
    const int o2 = g4 == 3 ? 256 : 2 * s0;
    *(LDS u16*)(kp + 2 * b0) = (u16)a01;
    *(LDS u16*)(kp + 2 * (b0 + s0)) = (u16)(a01 >> 16);
    *(LDS u16*)(kp + 2 * (b0 + o2)) = (u16)a23;
    *(LDS u16*)(kp + 2 * (b0 + o2 + s0)) = (u16)(a23 >> 16);
    if (g4 == 0) {
      *(LDS u16*)(kp + 2048) = (u16)c01; *(LDS u16*)(kp + 2560) = (u16)(c01 >> 16);
      *(LDS u16*)(kp + 3072) = (u16)c23; *(LDS u16*)(kp + 3584) = (u16)(c23 >> 16);
    }
    if (l == 16 && eh == 0) *(LDS u16*)(kp + 4096) = (u16)c01;
    BAR();
#pragma unroll
    for (int c = 0; c < 2; c++) {
      u32 v[11];
#pragma unroll
      for (int e = 0; e < 11; e++) v[e] = *(LDS u16*)(sm + L_KPOS + c * KPC + kaddr[e]);
      u32 od[5], ev[5];
#pragma unroll
      for (int k = 0; k < 5; k++) { od[k] = v[2 * k] | (v[2 * k + 1] << 16); ev[k] = v[2 * k + 1] | (v[2 * k + 2] << 16); }
      LDS char* kc = sm + L_KC + c * KCC + 32 + tid * 16;
      *(LDS u32x4*)(kc) = u32x4{ev[1], ev[2], ev[3], ev[4]};
      *(LDS u32x4*)(kc + KCP) = u32x4{od[1], od[2], od[3], od[4]};
      *(LDS u32x4*)(kc + 2 * KCP) = u32x4{ev[0], ev[1], ev[2], ev[3]};
      *(LDS u32x4*)(kc + 3 * KCP) = u32x4{od[0], od[1], od[2], od[3]};
    }
    const u32x4 zz = {0, 0, 0, 0};
    if (tid < 16) *(LDS u32x4*)(sm + L_KC + (tid >> 1) * KCP + (tid & 1) * 16) = zz;
    if (w == 1) *(LDS u32x4*)(sm + L_UI + (l >> 5) * UIC + (l & 31) * 16) = zz;
  }
  STAMP(5);
  u32x4 W1[2][16];
  WAITV(0);
  BAR();
  {
    LDS char* pw = sm + L_ST + (4 * g4 + q4) * 224 + 64 * w + 8 * p4;
#pragma unroll
    for (int ks = 0; ks < 13; ks++) {
      W1[0][ks] = tr_frag(pw + ks * 7168, pw + ks * 7168 + 16 * 224);
      W1[1][ks] = tr_frag(pw + ks * 7168 + 32, pw + ks * 7168 + 16 * 224 + 32);
    }
  }
  STAMP(6);
  u32 stat[8];
  {
    const u32 vo = WS_REC + tid * 64;
#pragma unroll 1
    for (int spin = 0; spin < kSpin; spin++) {
      u32x4 r0, r1, r2, r3;
      asm volatile("buffer_load_dwordx4 %0, %4, %5, 0 offen sc1\n\tbuffer_load_dwordx4 %1, %4, %5, 0 offen offset:16 sc1\n\t"
                   "buffer_load_dwordx4 %2, %4, %5, 0 offen offset:32 sc1\n\tbuffer_load_dwordx4 %3, %4, %5, 0 offen offset:48 sc1\n\ts_waitcnt vmcnt(0)"
                   : "=&v"(r0), "=&v"(r1), "=&v"(r2), "=&v"(r3) : "v"(vo), "s"(rWS) : "memory");
      stat[0] = r0.x; stat[1] = r0.z; stat[2] = r1.x; stat[3] = r1.z; stat[4] = r2.x; stat[5] = r2.z; stat[6] = r3.x; stat[7] = r3.z;
      const bool ok = r0.y == tag && r0.w == tag && r1.y == tag && r1.w == tag && r2.y == tag && r2.w == tag && r3.y == tag && r3.w == tag;
      if (__all(ok)) break;
      __builtin_amdgcn_s_sleep(2);
    }
  }
  BAR();
  STAMP(7);
  u32x4 xa, xb;
  u32x2 xr[4];
  {
    const u32 vo = WS_XT + 2 * id * 4096 + tid * 16;
    const u32 vr = WS_XT + (2 * id + ch) * 4096 + eh * 2048 + c16 * 32 + g4 * 8;
    const u32 k4096 = 4096;
    asm volatile("buffer_load_dwordx4 %0, %6, %8, 0 offen sc1\n\tbuffer_load_dwordx4 %1, %6, %8, %9 offen sc1\n\t"
                 "buffer_load_dwordx2 %2, %7, %8, 0 offen sc1\n\tbuffer_load_dwordx2 %3, %7, %8, 0 offen offset:512 sc1\n\t"
                 "buffer_load_dwordx2 %4, %7, %8, 0 offen offset:1024 sc1\n\tbuffer_load_dwordx2 %5, %7, %8, 0 offen offset:1536 sc1"
                 : "=&v"(xa), "=&v"(xb), "=&v"(xr[0]), "=&v"(xr[1]), "=&v"(xr[2]), "=&v"(xr[3]) : "v"(vo), "v"(vr), "s"(rWS), "s"(k4096) : "memory");
  }
  const u32 w2off = (l >> 3) * 1024 + ct * 128 + (((l & 7) ^ (((l >> 4) & 3) << 1)) << 4);
  const u32 w2n = (16 * ch + 8 * eh) * 1024, w2p = (112 * ch + 8 * eh) * 1024;
  {
    const int k1 = w < 3 ? w : 2;
#pragma unroll
    for (int i = 0; i < 7; i++) dma16(lds0 + L_S3 + k1 * 7168 + i * 1024, w1off[i], rW1, (13 + k1) * 32 * 3584);
#pragma unroll
    for (int i = 0; i < 18; i++) dma16(lds0 + L_S3 + 3 * 7168 + i * 4096 + w * 1024, w2off, rW2, f0_of(ks_of(i & 1, i >> 1)) * 1024 + w2n);
  }
  float mu[8], rs[8];
#pragma unroll
  for (int i = 0; i < 8; i++) { mu[i] = bf_lo(stat[i]); rs[i] = __builtin_amdgcn_rcpf(bf_hi(stat[i])); }
  asm volatile("s_waitcnt vmcnt(29)" : "+v"(xa), "+v"(xb)::"memory");
  STAMP(8);
  {
    u32x4 ua, ub;
#pragma unroll
    for (int k = 0; k < 4; k++) {
      ua[k] = pk_bf16((bf_lo(xa[k]) - mu[2 * k]) * rs[2 * k], (bf_hi(xa[k]) - mu[2 * k + 1]) * rs[2 * k + 1]);
      ub[k] = pk_bf16((bf_lo(xb[k]) - mu[2 * k]) * rs[2 * k], (bf_hi(xb[k]) - mu[2 * k + 1]) * rs[2 * k + 1]);
    }
    *(LDS u32x4*)(sm + L_UI + 512 + tid * 16) = ua;
    *(LDS u32x4*)(sm + L_UI + UIC + 512 + tid * 16) = ub;
  }
  BAR();
  STAMP(9);
  {
    f32x4 acc[8];
#pragma unroll
    for (int t = 0; t < 8; t++) acc[t] = f32x4{0, 0, 0, 0};
    LDS char* pu = sm + L_UI + ch * UIC + 480 + 32 * c16 + 16 * g4 - 256 * eh;
    LDS char* pk = sm + L_KC + ch * KCC + q4 * KCP + 56 + 8 * p4 - 16 * g4 + 256 * eh;
#pragma unroll 1
    for (int e = 0; e < 4; e++) {
      u32x4 B[8];
#pragma unroll
      for (int t = 0; t < 8; t++) B[t] = ld128(pu - 64 * e + 512 * t);
#pragma unroll
      for (int i = 0; i < 8; i++) {
        const u32x4 A = tr_frag(pk + 64 * e + 512 * i + 8, pk + 64 * e + 512 * i);
#pragma unroll
        for (int t = i; t < 8; t++) acc[t] = mfma16(A, B[t - i], acc[t]);
      }
    }
    STAMP(10);
    LDS char* ex = sm + L_EX + l * 16;
#pragma unroll
    for (int t = 0; t < 4; t++) *(LDS f32x4*)(ex + w * 4096 + t * 1024) = sel4(eh, acc[t], acc[4 + t]);
    BAR();
    asm volatile("s_waitcnt vmcnt(25)" : "+v"(xr[0]), "+v"(xr[1]), "+v"(xr[2]), "+v"(xr[3])::"memory");
    const u32 vh = WS_HT + (2 * id + ch) * 4096 + eh * 2048 + c16 * 32 + g4 * 8;
#pragma unroll
    for (int t = 0; t < 4; t++) {
      const f32x4 y = sel4(eh, acc[4 + t], acc[t]) + *(LDS f32x4*)(ex + (w ^ 1) * 4096 + t * 1024);
      const u32x2 h = {pk_bf16(bf_lo(xr[t].x) + y[0], bf_hi(xr[t].x) + y[1]), pk_bf16(bf_lo(xr[t].y) + y[2], bf_hi(xr[t].y) + y[3])};
      __builtin_amdgcn_raw_buffer_store_b64(h, rWS, vh + t * 512, 0, 16);
    }
  }
  STAMP(11);
  WAITV(0);
  BAR();
  if (tid == 0) __builtin_amdgcn_raw_buffer_store_b32(tag, rWS, WS_FLAG2 + id * 16, 0, 16);
  if (id == 0 && tid == 64) __builtin_amdgcn_raw_buffer_store_b32(tag, rWS, 0, 0, 16);
  STAMP(12);
  u32x4 W2[28][2];
  const u32 w2rd = (4 * g4 + q4) * 128 + (((4 * eh + (p4 >> 1)) ^ (((2 * g4 + (q4 >> 1)) & 3) << 1)) << 4) + 8 * (p4 & 1);
  f32x4 cs[2] = {{0, 0, 0, 0}, {0, 0, 0, 0}};
  {
    LDS char* pw = sm + L_S3 + (4 * g4 + q4) * 224 + 64 * w + 8 * p4;
#pragma unroll
    for (int ks = 13; ks < 16; ks++) {
      W1[0][ks] = tr_frag(pw + (ks - 13) * 7168, pw + (ks - 13) * 7168 + 16 * 224);
      W1[1][ks] = tr_frag(pw + (ks - 13) * 7168 + 32, pw + (ks - 13) * 7168 + 16 * 224 + 32);
    }
    LDS char* p2 = sm + L_S3 + 3 * 7168 + ch * 4096;
#pragma unroll
    for (int i = 0; i < 9; i++) {
      W2[i][0] = tr_frag(p2 + i * 8192 + w2rd, p2 + i * 8192 + 2048 + w2rd);
      W2[i][1] = tr_frag(p2 + i * 8192 + (w2rd ^ 32), p2 + i * 8192 + 2048 + (w2rd ^ 32));
    }
    const u32x4 ones = {ONES, ONES, ONES, ONES};
    if (w == 3) {
#pragma unroll
      for (int ks = 0; ks < 16; ks++) W1[1][ks] = ones;
    }
#pragma unroll
    for (int ks = 0; ks < 16; ks++) { cs[0] = mfma16(W1[0][ks], ones, cs[0]); cs[1] = mfma16(W1[1][ks], ones, cs[1]); }
  }
  STAMP(13);
  {
    const u32 vo = WS_FLAG2 + tid * 16;
#pragma unroll 1
    for (int spin = 0; spin < kSpin; spin++) {
      u32 f;
      asm volatile("buffer_load_dword %0, %1, %2, 0 offen sc1\n\ts_waitcnt vmcnt(0)" : "=&v"(f) : "v"(vo), "s"(rWS) : "memory");
      if (__all(f == tag)) break;
      __builtin_amdgcn_s_sleep(2);
    }
  }
  BAR();
  STAMP(14);
  u32x4 res[2];
  float rstd[2];
  {
    const u32 vo = WS_HT + (l >> 4) * 4096 + grp * 256;
    const u32 v0 = vo + ((c16 ^ ((l >> 4) << 1)) << 4), v1 = vo + ((c16 ^ ((4 + (l >> 4)) << 1)) << 4);
#pragma unroll
    for (int c = 0; c < 8; c++)
#pragma unroll
      for (int i = 0; i < 4; i++) dma16c(lds0 + L_HT + (64 * c + 16 * w + 4 * i) * 256, (i & 1) ? v1 : v0, rWS, (64 * c + 16 * w + 4 * i) * 4096);
    const int gt = 4 * half + w;
    const int sw = 4 * (g4 & 1) + q4;
    const u32 rowoff = L_HT + (4 * g4 + q4) * 256 + (p4 >> 1) * 16 + (p4 & 1) * 8;
    LDS char* pb[8];
#pragma unroll
    for (int r = 0; r < 8; r++) pb[r] = sm + rowoff + ((gt ^ sw ^ r) << 5);
    f32x4 acc[8][2], gram = {0, 0, 0, 0};
#pragma unroll
    for (int r = 0; r < 8; r++) { acc[r][0] = f32x4{0, 0, 0, 0}; acc[r][1] = f32x4{0, 0, 0, 0}; }
#pragma unroll
    for (int c = 0; c < 8; c++) {
      if (c == 0) WAITV(28); if (c == 1) WAITV(24); if (c == 2) WAITV(20); if (c == 3) WAITV(16);
      if (c == 4) WAITV(12); if (c == 5) WAITV(8); if (c == 6) WAITV(4); if (c == 7) WAITV(0);
      asm volatile("s_barrier" ::: "memory");
      if (c == 0) STAMP(15);
      if (c == 4) {
#pragma unroll
        for (int r = 0; r < 8; r++) pb[r] += 65536;
      }
#pragma unroll
      for (int k2 = 0; k2 < 2; k2++) {
        const int ks = 2 * c + k2, o = (ks & 7) * 8192;
#pragma unroll
        for (int r = 0; r < 8; r++) {
          const u32x4 hb = tr_frag(pb[r] + o, pb[r] + o + 4096);
          acc[r][0] = mfma16(W1[0][ks], hb, acc[r][0]);
          acc[r][1] = mfma16(W1[1][ks], hb, acc[r][1]);
          if (r == 0) gram = mfma16(hb, hb, gram);
        }
      }
    }
    STAMP(16);
    {
      LDS char* pr = sm + rowoff + (2 * ct + eh) * 8192;
      const int rt = 4 * half + 2 * ch;
      res[0] = tr_frag(pr + ((rt ^ sw) << 5), pr + 4096 + ((rt ^ sw) << 5));
      res[1] = tr_frag(pr + (((rt + 1) ^ sw) << 5), pr + 4096 + (((rt + 1) ^ sw) << 5));
      const float qv = pick4(gram, p4);
      if (q4 == g4) *(LDS float*)(sm + L_SQ + (w * 16 + c16) * 4) = qv;
      if (w == 3 && g4 == 0) {
#pragma unroll
        for (int r = 0; r < 8; r++) *(LDS float*)(sm + L_SUM + ((gt ^ r) * 16 + c16) * 4) = acc[r][1][0];
      }
    }
    BAR();
    const u32 vb = WS_H1 + l * 16, vs = WS_H1S + l * 16;
    u32x2 keep = {0, 0};
#pragma unroll
    for (int r = 0; r < 8; r++) {
      const float m = -rbf(*(LDS float*)(sm + L_SUM + ((gt ^ r) * 16 + c16) * 4) * (1.f / 512));
      const f32x4 z0 = acc[r][0] + m * cs[0], z1 = acc[r][1] + m * cs[1];
      u32x4 o = {pk_bf16(z0[0], z0[1]), pk_bf16(z0[2], z0[3]), pk_bf16(z1[0], z1[1]), pk_bf16(z1[2], z1[3])};
#pragma unroll
      for (int k = 0; k < 4; k++) o[k] = relu_pk(o[k]);
      if (w < 3) {
        __builtin_amdgcn_raw_buffer_store_b128(o, rWS, vb, ((8 * grp + (gt ^ r)) * 48 + 3 * jt + w) * 1024, 16);
      } else if (r & 1) {
        __builtin_amdgcn_raw_buffer_store_b128(u32x4{o[0], o[1], keep.x, keep.y}, rWS, vs, (jt * 64 + 4 * grp + ((gt ^ r) >> 1)) * 1024, 16);
      } else {
        keep = u32x2{o[0], o[1]};
      }
    }
#pragma unroll
    for (int k = 0; k < 2; k++) {
      const float m = *(LDS float*)(sm + L_SUM + ((4 * half + 2 * ch + k) * 16 + c16) * 4) * (1.f / 512);
      const float var = fmaxf(*(LDS float*)(sm + L_SQ + ((2 * ch + k) * 16 + c16) * 4) * (1.f / 512) - m * m, 0.f);
      rstd[k] = __builtin_amdgcn_rcpf(rbf(__builtin_amdgcn_sqrtf(rbf(rbf(var) + 1e-5f))));
    }
  }
  STAMP(17);
#pragma unroll
  for (int i = 0; i < 38; i++) {
    const int ks = ks_of(i & 1, 9 + (i >> 1));
    dma16(lds0 + L_HT + i * 4096 + w * 1024, w2off, rW2, f0_of(ks) * 1024 + (ks < 48 ? w2n : w2p));
  }
  WAITV(38);
  asm volatile("s_barrier" ::: "memory");
  if (tid == 0) __builtin_amdgcn_raw_buffer_store_b32(tag, rWS, WS_FLAG3 + id * 16, 0, 16);
  STAMP(18);
  WAITV(0);
  asm volatile("s_barrier" ::: "memory");
  {
    LDS char* p2 = sm + L_HT + ch * 4096;
#pragma unroll
    for (int i = 9; i < 28; i++) {
      W2[i][0] = tr_frag(p2 + (i - 9) * 8192 + w2rd, p2 + (i - 9) * 8192 + 2048 + w2rd);
      W2[i][1] = tr_frag(p2 + (i - 9) * 8192 + (w2rd ^ 32), p2 + (i - 9) * 8192 + 2048 + (w2rd ^ 32));
    }
  }
  STAMP(19);
  {
    const u32 vo = WS_FLAG3 + (16 * grp + c16) * 16;
#pragma unroll 1
    for (int spin = 0; spin < kSpin; spin++) {
      u32 f;
      asm volatile("buffer_load_dword %0, %1, %2, 0 offen sc1\n\ts_waitcnt vmcnt(0)" : "=&v"(f) : "v"(vo), "s"(rWS) : "memory");
      if (__all(f == tag)) break;
      __builtin_amdgcn_s_sleep(2);
    }
  }
  BAR();
  STAMP(20);
  {
    const u32 vl = l * 16;
    const int rt0 = 8 * grp + 4 * half;
    const u32 sn = WS_H1 + (rt0 * 48 + 24 * ch + eh) * 1024;
    const u32 sp = WS_H1S + ((8 * ch + 2 * eh) * 64 + (rt0 >> 1)) * 1024;
#define ISSUE(s)                                                                                                        \
  _Pragma("unroll") for (int r = 0; r < 4; r++)                                                                         \
      dma16c(lds0 + L_HT + ((s) % 9) * 16384 + w * 4096 + r * 1024, vl, rWS,                                            \
             (s) < 12 ? sn + (r * 48 + 2 * (s)) * 1024 : sp + ((4 * ((s) - 12) + (r >> 1)) * 64 + (r & 1)) * 1024)
#pragma unroll
    for (int s = 0; s < 9; s++) { ISSUE(s); }
    f32x4 acc[4][2];
#pragma unroll
    for (int r = 0; r < 4; r++) { acc[r][0] = f32x4{0, 0, 0, 0}; acc[r][1] = f32x4{0, 0, 0, 0}; }
    LDS char* ph = sm + L_HT + ch * 8192 + l * 16;
#pragma unroll
    for (int s = 0; s < 14; s++) {
      if (s == 0) WAITV(32); else if (s <= 6) WAITV(28);
      if (s == 7) WAITV(24); if (s == 8) WAITV(20); if (s == 9) WAITV(16); if (s == 10) WAITV(12);
      if (s == 11) WAITV(8); if (s == 12) WAITV(4); if (s == 13) WAITV(0);
      BAR();
      if (s == 0) STAMP(21);
      if (s >= 1 && s <= 5) { ISSUE(s + 8); }
#pragma unroll
      for (int k2 = 0; k2 < 2; k2++) {
#pragma unroll
        for (int r = 0; r < 4; r++) {
          u32x4 hb;
          LDS char* pp = ph + (s % 9) * 16384 + k2 * 4096;
          if (s < 12) hb = ld128(pp + r * 1024);
          else {
            const u32x2 lo = *(LDS u32x2*)(pp + (r >> 1) * 1024 + (r & 1) * 8), hi = *(LDS u32x2*)(pp + 2048 + (r >> 1) * 1024 + (r & 1) * 8);
            hb = u32x4{lo.x, lo.y, hi.x, hi.y};
          }
          acc[r][0] = mfma16(W2[2 * s + k2][0], hb, acc[r][0]);
          acc[r][1] = mfma16(W2[2 * s + k2][1], hb, acc[r][1]);
        }
      }
    }
#undef ISSUE
    STAMP(22);
    LDS char* ex = sm + L_HT + 5 * 16384 + l * 16;
#pragma unroll
    for (int k = 0; k < 2; k++)
#pragma unroll
      for (int t = 0; t < 2; t++) *(LDS f32x4*)(ex + w * 4096 + (2 * k + t) * 1024) = sel4(ch, acc[k][t], acc[2 + k][t]);
    BAR();
    const rsrc_t rO = mk_rsrc(out, 2u << 20);
#pragma unroll
    for (int k = 0; k < 2; k++) {
      u32 o[2][2];
#pragma unroll
      for (int t = 0; t < 2; t++) {
        const f32x4 y = (sel4(ch, acc[2 + k][t], acc[k][t]) + *(LDS f32x4*)(ex + (w ^ 2) * 4096 + (2 * k + t) * 1024)) * rstd[k];
        o[t][0] = pk_bf16(bf_lo(res[k][2 * t]) + y[0], bf_hi(res[k][2 * t]) + y[1]);
        o[t][1] = pk_bf16(bf_lo(res[k][2 * t + 1]) + y[2], bf_hi(res[k][2 * t + 1]) + y[3]);
      }
      const auto s0 = __builtin_amdgcn_permlane16_swap(o[0][0], o[1][0], false, false);
      const auto s1 = __builtin_amdgcn_permlane16_swap(o[0][1], o[1][1], false, false);
      const u32 vo = ((rt0 + 2 * ch + k) * 16 + c16) * 1024 + ct * 128 + eh * 64 + (g4 & 1) * 32 + (g4 >> 1) * 16;
      __builtin_amdgcn_raw_buffer_store_b128(u32x4{s0[0], s1[0], s0[1], s1[1]}, rO, vo, 0, 0);
    }
  }
}

extern "C" void kernel_launch(void* const* d_in, const int* in_sizes, int n_in, void* d_out, int out_size, void* d_ws, size_t ws_size,
                              hipStream_t stream) {
  (void)hipFuncSetAttribute((const void*)fused_block, hipFuncAttributeMaxDynamicSharedMemorySize, LDS_BYTES);
  fused_block<<<256, 256, LDS_BYTES, stream>>>((const u16*)d_in[0], (const u16*)d_in[1], (const u16*)d_in[2], (const u16*)d_in[3],
                                               (const u16*)d_in[4], (const u32*)d_in[5], (const u32*)d_in[6], (const u16*)d_in[8],
                                               (const u16*)d_in[9], (u16*)d_out, (char*)d_ws);
}
```

```cpp
#include <hip/hip_runtime.h>
#include <stdint.h>
typedef unsigned short u16;
typedef unsigned u32;
constexpr u32 WS_XT = 1u << 20, WS_HT = 4u << 20, WS_H1 = 8u << 20, WS_STAMP = 32u << 20, TP = 4352;
constexpr int LDS_BYTES = 163840;

__global__ __launch_bounds__(256, 1) void fused_block(const u16* x, const u16* p_la, const u16* p_ai, const u16* p_cr, const u16* p_ci,
                                                      const u16* p_ldt, const u16* p_dsk, const u16* w1, const u16* w2, u16* out, char* ws) {
  asm volatile(
      "s_load_dwordx2 s[4:5], s[0:1], 0x0\n\t"
      "s_load_dwordx2 s[8:9], s[0:1], 0x50\n\t"
      "s_load_dwordx2 s[12:13], s[0:1], 0x8\n\t"
      "s_load_dwordx2 s[16:17], s[0:1], 0x10\n\t"
      "s_load_dwordx2 s[20:21], s[0:1], 0x18\n\t"
      "s_load_dwordx2 s[24:25], s[0:1], 0x20\n\t"
      "s_load_dwordx4 s[40:43], s[0:1], 0x28\n\t"
      "s_load_dwordx2 s[28:29], s[0:1], 0x38\n\t"
      "s_load_dwordx2 s[32:33], s[0:1], 0x40\n\t"
      "s_load_dwordx2 s[36:37], s[0:1], 0x48\n\t"
      "v_readfirstlane_b32 s45, v0\n\t"
      "s_and_b32 s44, s2, 7\n\t"
      "s_lshr_b32 s55, s2, 3\n\t"
      "s_lshl_b32 s44, s44, 5\n\t"
      "s_add_u32 s44, s44, s55\n\t"
      "s_lshr_b32 s45, s45, 6\n\t"
      "v_and_b32 v1, 63, v0\n\t"
      "s_mov_b32 s7, 0x20000\n\t"
      "s_mov_b32 s6, 0x200000\n\t"
      "s_mov_b32 s11, s7\n\t"
      "s_mov_b32 s10, 0x4000000\n\t"
      "s_mov_b32 s14, s7\n\t"
      "s_mov_b32 s15, s7\n\t"
      "s_mov_b32 s18, s7\n\t"
      "s_mov_b32 s19, s7\n\t"
      "s_mov_b32 s22, s7\n\t"
      "s_mov_b32 s23, s7\n\t"
      "s_mov_b32 s26, s7\n\t"
      "s_mov_b32 s27, s7\n\t"
      "v_and_b32 v64, 7, v0\n\t"
      "v_bfe_u32 v65, v0, 3, 3\n\t"
      "v_lshlrev_b32 v66, 13, v64\n\t"
      "v_lshl_or_b32 v66, v65, 3, v66\n\t"
      "v_lshlrev_b32 v67, 10, v64\n\t"
      "v_lshl_or_b32 v67, v65, 4, v67\n\t"
      "v_lshlrev_b32 v68, 1, v1\n\t"
      "s_lshr_b32 s55, s44, 3\n\t"
      "s_and_b32 s56, s44, 7\n\t"
      "s_lshl_b32 s68, s55, 16\n\t"
      "s_lshl_b32 s57, s56, 7\n\t"
      "s_add_u32 s68, s68, s57\n\t"
      "s_lshl_b32 s57, s45, 6\n\t"
      "s_add_u32 s68, s68, s57\n\t"
      "s_add_u32 s69, s68, 0x1000\n\t"
      "s_lshl_b32 s70, s44, 13\n\t"
      "s_lshl_b32 s57, s45, 9\n\t"
      "s_add_u32 s70, s70, s57\n\t"
      "s_lshl_b32 s71, s44, 9\n\t"
      "s_lshl_b32 s57, s45, 7\n\t"
      "s_add_u32 s71, s71, s57\n\t"
      "s_lshl_b32 s58, s44, 2\n\t"
      "s_cmp_gt_u32 s45, 1\n\t"
      "s_waitcnt lgkmcnt(0)\n\t"
      "s_cbranch_scc1 Lp0b%=\n\t"
      "buffer_load_dword v128, off, s[8:11], 0 sc1\n\t"
      "buffer_load_dwordx2 v[80:81], v66, s[4:7], s68 offen\n\t"
      "buffer_load_dwordx2 v[82:83], v66, s[4:7], s68 offen offset:1024\n\t"
      "buffer_load_dwordx2 v[84:85], v66, s[4:7], s68 offen offset:2048\n\t"
      "buffer_load_dwordx2 v[86:87], v66, s[4:7], s68 offen offset:3072\n\t"
      "buffer_load_dwordx2 v[88:89], v66, s[4:7], s69 offen\n\t"
      "buffer_load_dwordx2 v[90:91], v66, s[4:7], s69 offen offset:1024\n\t"
      "buffer_load_dwordx2 v[92:93], v66, s[4:7], s69 offen offset:2048\n\t"
      "buffer_load_dwordx2 v[94:95], v66, s[4:7], s69 offen offset:3072\n\t"
      "buffer_load_dwordx4 v[96:99], v67, s[4:7], s70 offen\n\t"
      "buffer_load_dwordx4 v[100:103], v67, s[4:7], s70 offen offset:128\n\t"
      "buffer_load_dwordx4 v[104:107], v67, s[4:7], s70 offen offset:256\n\t"
      "buffer_load_dwordx4 v[108:111], v67, s[4:7], s70 offen offset:384\n\t"
      "Lp0b%=:\n\t"
      "buffer_load_ushort v129, v68, s[12:15], s71 offen\n\t"
      "buffer_load_ushort v130, v68, s[16:19], s71 offen\n\t"
      "buffer_load_ushort v131, v68, s[20:23], s71 offen\n\t"
      "buffer_load_ushort v132, v68, s[24:27], s71 offen\n\t"
      "s_load_dword s40, s[40:41], s58\n\t"
      "s_load_dword s42, s[42:43], s58\n\t"
      "s_mov_b32 s30, 0x1c0000\n\t"
      "s_mov_b32 s31, s7\n\t"
      "s_mov_b32 s34, s30\n\t"
      "s_mov_b32 s35, s7\n\t"
      "s_mov_b32 s38, s6\n\t"
      "s_mov_b32 s39, s7\n\t"
      "s_lshr_b32 s46, s45, 1\n\t"
      "s_and_b32 s47, s45, 1\n\t"
      "s_lshr_b32 s48, s44, 4\n\t"
      "s_and_b32 s49, s44, 15\n\t"
      "s_lshr_b32 s50, s49, 3\n\t"
      "s_and_b32 s51, s49, 7\n\t"
      "s_lshl_b32 s52, s45, 12\n\t"
      "s_lshl_b32 s54, s45, 13\n\t"
      "s_mul_i32 s76, s49, 224\n\t"
      "s_movk_i32 s59, 3360\n\t"
      "v_lshlrev_b32 v30, 4, v1\n\t"
      "v_mul_u32_u24 v69, 0x124a, v1\n\t"
      "v_add_u32 v70, 0x1000, v30\n\t"
      "v_add_u32 v57, 0x49280, v69\n\t"
      "v_add_u32 v58, 0x92500, v69\n\t"
      "v_add_u32 v59, 0xdb780, v69\n\t"
      "v_add_u32 v60, 0x124a00, v69\n\t"
      "v_add_u32 v61, 0x16dc80, v69\n\t"
      "v_add_u32 v62, 0x1b6f00, v69\n\t"
      "v_lshrrev_b32 v56, 16, v69\n\t"
      "v_lshrrev_b32 v57, 16, v57\n\t"
      "v_lshrrev_b32 v58, 16, v58\n\t"
      "v_lshrrev_b32 v59, 16, v59\n\t"
      "v_lshrrev_b32 v60, 16, v60\n\t"
      "v_lshrrev_b32 v61, 16, v61\n\t"
      "v_lshrrev_b32 v62, 16, v62\n\t"
      "v_mad_u32_u24 v56, v56, s59, v30\n\t"
      "v_mad_u32_u24 v57, v57, s59, v30\n\t"
      "v_mad_u32_u24 v58, v58, s59, v30\n\t"
      "v_mad_u32_u24 v59, v59, s59, v30\n\t"
      "v_mad_u32_u24 v60, v60, s59, v70\n\t"
      "v_mad_u32_u24 v61, v61, s59, v70\n\t"
      "v_mad_u32_u24 v62, v62, s59, v70\n\t"
      "s_mov_b32 s3, 0\n\t"
      "s_cmp_lt_u32 s45, 2\n\t"
      "s_cbranch_scc1 Lp0d%=\n\t"
      "s_getpc_b64 s[88:89]\n\t"
      "Lpc%=:\n\t"
      "s_mov_b32 s90, Lfin%=-Lpc%=\n\t"
      "s_mov_b32 s91, s7\n\t"
      "v_lshlrev_b32 v71, 7, v1\n\t"
      "s_and_b32 s56, s44, 31\n\t"
      "s_lshl_b32 s56, s56, 10\n\t"
      "s_lshl_b32 s57, s47, 9\n\t"
      "s_add_u32 s56, s56, s57\n\t"
      "s_mov_b64 exec, 0xf\n\t"
      "buffer_load_dword v63, v71, s[88:91], s56 offen\n\t"
      "s_mov_b64 exec, -1\n\t"
      "v_lshlrev_b32 v71, 7, v1\n\t"
      "s_mul_i32 s56, s44, 7168\n\t"
      "s_mul_i32 s57, s47, 3584\n\t"
      "s_add_u32 s56, s56, s57\n\t"
      "s_mov_b64 exec, 0xfffffff\n\t"
      "buffer_load_dword v63, v71, s[28:31], s56 offen\n\t"
      "s_mov_b64 exec, -1\n\t"
      "s_mul_i32 s56, s47, 0\n\t"
      "s_add_u32 s56, s56, 0x11000\n\t"
      "s_mul_i32 s57, s47, 0\n\t"
      "s_add_u32 s57, s57, s76\n\t"
      "Lp0d%=:\n\t"
      "v_lshrrev_b32 v212, 4, v1\n\t"
      "v_and_b32 v213, 15, v0\n\t"
      "v_lshlrev_b32 v21, 4, v0\n\t"
      "v_lshlrev_b32 v22, 4, v0\n\t"
      "v_mov_b32 v52, 0x3f803f80\n\t"
      "v_mov_b32 v53, v52\n\t"
      "v_mov_b32 v54, v52\n\t"
      "v_mov_b32 v55, v52\n\t"
      "v_and_b32 v72, 1, v0\n\t"
      "v_and_b32 v73, 2, v0\n\t"
      "v_bfe_u32 v74, v0, 2, 2\n\t"
      "v_cmp_ne_u32 s[60:61], 0, v72\n\t"
      "v_cmp_ne_u32 s[62:63], 0, v73\n\t"
      "v_cmp_eq_u32 s[64:65], v74, v212\n\t"
      "v_lshlrev_b32 v75, 4, v64\n\t"
      "s_movk_i32 s59, 17408\n\t"
      "v_mad_u32_u24 v50, v65, s59, v75\n\t"
      "s_and_b32 s56, s44, 7\n\t"
      "s_lshl_b32 s56, s56, 1\n\t"
      "s_add_u32 s56, s56, s45\n\t"
      "s_mul_i32 s68, s56, 139264\n\t"
      "s_lshr_b32 s56, s44, 3\n\t"
      "s_lshl_b32 s56, s56, 7\n\t"
      "s_add_u32 s68, s68, s56\n\t"
      "s_add_u32 s68, s68, 0x100000\n\t"
      "s_add_u32 s69, s68, 4352\n\t"
      "s_add_u32 s70, s68, 8704\n\t"
      "s_add_u32 s71, s68, 13056\n\t"
      "s_mov_b32 s74, 0x05040100\n\t"
      "s_mov_b32 s75, 0x07060302\n\t"
      "v_and_b32 v76, 8, v0\n\t"
      "v_lshl_or_b32 v76, v64, 5, v76\n\t"
      "s_lshl_b32 s56, s45, 4\n\t"
      "v_add_u32 v46, s56, v76\n\t"
      "v_lshlrev_b32 v47, 5, v1\n\t"
      "v_lshrrev_b32 v76, 1, v1\n\t"
      "v_and_b32 v77, 1, v0\n\t"
      "v_lshlrev_b32 v77, 3, v77\n\t"
      "v_lshl_or_b32 v48, v76, 12, v77\n\t"
      "s_cmp_lg_u32 s45, 3\n\t"
      "s_cbranch_scc1 Lp0e%=\n\t"
      "s_movk_i32 s59, 7184\n\t"
      "v_mul_u32_u24 v76, s59, v1\n\t"
      "v_add_u32 v76, 0x10000, v76\n\t"
      "v_mov_b32 v77, 0\n\t"
      "s_mov_b64 exec, 0x1fff\n\t"
      "ds_write_b128 v76, v[52:55] offset:11264\n\t"
      "s_mov_b64 exec, 1\n\t"
      "v_mov_b32 v76, 0xfe84\n\t"
      "ds_write_b32 v76, v77\n\t"
      "ds_write_b32 v76, v77 offset:4240\n\t"
      "s_mov_b64 exec, -1\n\t"
      "Lp0e%=:\n\t"
      "v_lshlrev_b32 v76, 3, v0\n\t"
      "v_cvt_f32_u32 v76, v76\n\t"
      "v_cmp_eq_u32 vcc, 0, v0\n\t"
      "v_add_f32 v140, 0xc0400000, v76\n\t"
      "v_add_f32 v141, 0xc0000000, v76\n\t"
      "v_add_f32 v142, 0xbf800000, v76\n\t"
      "v_mov_b32 v143, v76\n\t"
      "v_add_f32 v144, 0x3f800000, v76\n\t"
      "v_add_f32 v145, 0x40000000, v76\n\t"
      "v_add_f32 v146, 0x40400000, v76\n\t"
      "v_add_f32 v147, 0x40800000, v76\n\t"
      "v_add_f32 v148, 0x40a00000, v76\n\t"
      "v_add_f32 v149, 0x40c00000, v76\n\t"
      "v_add_f32 v150, 0x40e00000, v76\n\t"
      "v_cvt_pk_bf16_f32 v152, v140, v141\n\t"
      "v_cvt_pk_bf16_f32 v153, v142, v143\n\t"
      "v_cvt_pk_bf16_f32 v154, v144, v145\n\t"
      "v_cvt_pk_bf16_f32 v155, v146, v147\n\t"
      "v_cvt_pk_bf16_f32 v156, v148, v149\n\t"
      "v_cvt_pk_bf16_f32 v157, v150, v150\n\t"
      "v_lshlrev_b32 v140, 16, v152\n\t"
      "v_and_b32 v141, 0xffff0000, v152\n\t"
      "v_lshlrev_b32 v142, 16, v153\n\t"
      "v_and_b32 v143, 0xffff0000, v153\n\t"
      "v_lshlrev_b32 v144, 16, v154\n\t"
      "v_and_b32 v145, 0xffff0000, v154\n\t"
      "v_lshlrev_b32 v146, 16, v155\n\t"
      "v_and_b32 v147, 0xffff0000, v155\n\t"
      "v_lshlrev_b32 v148, 16, v156\n\t"
      "v_and_b32 v149, 0xffff0000, v156\n\t"
      "v_lshlrev_b32 v150, 16, v157\n\t"
      "v_cvt_u32_f32 v140, v140\n\t"
      "v_cvt_u32_f32 v141, v141\n\t"
      "v_cvt_u32_f32 v142, v142\n\t"
      "v_cvt_u32_f32 v143, v143\n\t"
      "v_cvt_u32_f32 v144, v144\n\t"
      "v_cvt_u32_f32 v145, v145\n\t"
      "v_cvt_u32_f32 v146, v146\n\t"
      "v_cvt_u32_f32 v147, v147\n\t"
      "v_cvt_u32_f32 v148, v148\n\t"
      "v_cvt_u32_f32 v149, v149\n\t"
      "v_cvt_u32_f32 v150, v150\n\t"
      "v_mov_b32 v77, 2050\n\t"
      "v_cndmask_b32 v140, v140, v77, vcc\n\t"
      "v_cndmask_b32 v141, v141, v77, vcc\n\t"
      "v_cndmask_b32 v142, v142, v77, vcc\n\t"
      "v_lshrrev_b32 v152, 4, v140\n\t"
      "v_lshrrev_b32 v153, 4, v141\n\t"
      "v_lshrrev_b32 v154, 4, v142\n\t"
      "v_lshrrev_b32 v155, 4, v143\n\t"
      "v_lshrrev_b32 v156, 4, v144\n\t"
      "v_lshrrev_b32 v157, 4, v145\n\t"
      "v_lshrrev_b32 v158, 4, v146\n\t"
      "v_lshrrev_b32 v159, 4, v147\n\t"
      "v_lshrrev_b32 v160, 4, v148\n\t"
      "v_lshrrev_b32 v161, 4, v149\n\t"
      "v_lshrrev_b32 v162, 4, v150\n\t"
      "v_and_b32 v152, 0xfc, v152\n\t"
      "v_and_b32 v153, 0xfc, v153\n\t"
      "v_and_b32 v154, 0xfc, v154\n\t"
      "v_and_b32 v155, 0xfc, v155\n\t"
      "v_and_b32 v156, 0xfc, v156\n\t"
      "v_and_b32 v157, 0xfc, v157\n\t"
      "v_and_b32 v158, 0xfc, v158\n\t"
      "v_and_b32 v159, 0xfc, v159\n\t"
      "v_and_b32 v160, 0xfc, v160\n\t"
      "v_and_b32 v161, 0xfc, v161\n\t"
      "v_and_b32 v162, 0xfc, v162\n\t"
      "v_lshl_add_u32 v2, v140, 1, v152\n\t"
      "v_lshl_add_u32 v3, v141, 1, v153\n\t"
      "v_lshl_add_u32 v4, v142, 1, v154\n\t"
      "v_lshl_add_u32 v5, v143, 1, v155\n\t"
      "v_lshl_add_u32 v6, v144, 1, v156\n\t"
      "v_lshl_add_u32 v7, v145, 1, v157\n\t"
      "v_lshl_add_u32 v8, v146, 1, v158\n\t"
      "v_lshl_add_u32 v9, v147, 1, v159\n\t"
      "v_lshl_add_u32 v10, v148, 1, v160\n\t"
      "v_lshl_add_u32 v11, v149, 1, v161\n\t"
      "v_lshl_add_u32 v12, v150, 1, v162\n\t"
      "s_mul_i32 s56, s46, 28832\n\t"
      "s_add_u32 s56, s56, 2048\n\t"
      "v_lshl_add_u32 v76, v212, 4, s56\n\t"
      "s_movk_i32 s59, 544\n\t"
      "v_mad_u32_u24 v19, v213, s59, v76\n\t"
      "s_mul_i32 s57, s47, 8704\n\t"
      "v_add_u32 v18, s57, v19\n\t"
      "v_min_u32 v77, 4, v213\n\t"
      "v_mad_u32_u24 v20, v77, s59, v76\n\t"
      "s_lshl_b32 s57, s47, 8\n\t"
      "s_add_u32 s87, s56, s57\n\t"
      "s_lshl_b32 s57, s47, 7\n\t"
      "v_lshl_add_u32 v76, v213, 3, s57\n\t"
      "v_cmp_gt_u32 vcc, 2, v212\n\t"
      "v_lshlrev_b32 v77, 2, v212\n\t"
      "v_lshlrev_b32 v78, 8, v212\n\t"
      "v_add_u32 v78, 0xffffff00, v78\n\t"
      "v_cndmask_b32 v77, v78, v77, vcc\n\t"
      "v_add_u32 v77, v76, v77\n\t"
      "v_max_u32 v78, 1, v212\n\t"
      "v_lshlrev_b32 v78, v78, 1\n\t"
      "v_cmp_eq_u32 vcc, 3, v212\n\t"
      "v_mov_b32 v79, 0x100\n\t"
      "s_nop 0\n\t"
      "v_cndmask_b32 v79, v78, v79, vcc\n\t"
      "v_add_u32 v79, v77, v79\n\t"
      "s_mul_i32 s56, s46, 4240\n\t"
      "s_add_u32 s56, s56, 60928\n\t"
      "v_lshrrev_b32 v140, 4, v77\n\t"
      "v_and_b32 v140, 0xfc, v140\n\t"
      "v_lshl_add_u32 v140, v77, 1, v140\n\t"
      "v_add_u32 v13, s56, v140\n\t"
      "v_lshrrev_b32 v140, 4, v79\n\t"
      "v_and_b32 v140, 0xfc, v140\n\t"
      "v_lshl_add_u32 v140, v79, 1, v140\n\t"
      "v_add_u32 v15, s56, v140\n\t"
      "v_lshrrev_b32 v140, 4, v76\n\t"
      "v_and_b32 v140, 0xfc, v140\n\t"
      "v_lshl_add_u32 v140, v76, 1, v140\n\t"
      "v_add_u32 v17, s56, v140\n\t"
      "v_add_u32 v14, v13, v78\n\t"
      "v_add_u32 v16, v15, v78\n\t"
      "v_cmp_eq_u32 vcc, 16, v1\n\t"
      "v_add_u32 v140, 2112, v17\n\t"
      "s_nop 0\n\t"
      "v_cndmask_b32 v17, v17, v140, vcc\n\t"
      "s_mov_b32 s66, 0xffff\n\t"
      "s_cmp_eq_u32 s47, 0\n\t"
      "s_mov_b32 s67, 0\n\t"
      "s_cselect_b32 s66, 0x1ffff, s66\n\t"
      "v_lshrrev_b32 v76, 5, v1\n\t"
      "v_lshlrev_b32 v76, 2, v76\n\t"
      "v_add_u32 v76, v76, v74\n\t"
      "s_movk_i32 s59, 448\n\t"
      "v_and_b32 v77, 3, v0\n\t"
      "v_and_b32 v78, 16, v0\n\t"
      "v_lshlrev_b32 v77, 3, v77\n\t"
      "v_lshl_or_b32 v77, v78, 1, v77\n\t"
      "v_mad_u32_u24 v76, v76, s59, v77\n\t"
      "s_lshl_b32 s56, s45, 6\n\t"
      "s_mul_i32 s57, s47, 3584\n\t"
      "s_add_u32 s56, s56, 0x11000\n\t"
      "s_add_u32 s58, s56, s57\n\t"
      "s_sub_u32 s56, s56, s57\n\t"
      "s_add_u32 s56, s56, 3584\n\t"
      "v_add_u32 v23, s58, v76\n\t"
      "v_add_u32 v25, s56, v76\n\t"
      "v_add_u32 v24, 224, v23\n\t"
      "v_add_u32 v26, 224, v25\n\t"
      "s_cmp_eq_u32 s45, 3\n\t"
      "s_cselect_b32 s56, 16, 32\n\t"
      "v_cmp_eq_u32 vcc, s56, v78\n\t"
      "v_mov_b32 v76, 0x12c00\n\t"
      "s_nop 0\n\t"
      "v_cndmask_b32 v23, v23, v76, vcc\n\t"
      "v_cndmask_b32 v24, v24, v76, vcc\n\t"
      "v_cndmask_b32 v25, v25, v76, vcc\n\t"
      "v_cndmask_b32 v26, v26, v76, vcc\n\t"
      "v_lshlrev_b32 v72, 3, v212\n\t"
      "v_lshl_or_b32 v27, v213, 5, v72\n\t"
      "s_mul_i32 s56, s46, 4608\n\t"
      "s_lshl_b32 s57, s47, 8\n\t"
      "s_sub_u32 s56, s56, s57\n\t"
      "s_add_u32 s56, s56, 35616\n\t"
      "v_add3_u32 v28, v27, v72, s56\n\t"
      "s_mul_i32 s56, s46, 16640\n\t"
      "s_add_u32 s56, s56, s57\n\t"
      "s_add_u32 s56, s56, 2104\n\t"
      "v_bfe_u32 v73, v0, 2, 2\n\t"
      "v_and_b32 v74, 3, v0\n\t"
      "v_lshl_add_u32 v75, v74, 3, s56\n\t"
      "v_lshlrev_b32 v76, 4, v212\n\t"
      "v_sub_u32 v75, v75, v76\n\t"
      "s_movk_i32 s59, 4160\n\t"
      "v_mad_u32_u24 v29, v73, s59, v75\n\t"
      "v_add_u32 v31, s52, v30\n\t"
      "v_xor_b32 v32, 0x1000, v31\n\t"
      "v_xor_b32 v33, 0x2000, v31\n\t"
      "v_bfe_u32 v75, v0, 4, 1\n\t"
      "v_and_b32 v76, 7, v0\n\t"
      "v_lshlrev_b32 v75, 2, v75\n\t"
      "v_xor_b32 v75, v75, v76\n\t"
      "v_lshlrev_b32 v75, 4, v75\n\t"
      "v_bfe_u32 v76, v0, 3, 3\n\t"
      "s_lshl_b32 s56, s51, 7\n\t"
      "v_lshl_add_u32 v77, v76, 10, s56\n\t"
      "v_add_u32 v34, v77, v75\n\t"
      "s_movk_i32 s59, 4352\n\t"
      "v_mad_u32_u24 v36, v76, s59, v75\n\t"
      "v_and_b32 v75, 16, v0\n\t"
      "v_and_b32 v76, 2, v0\n\t"
      "v_lshlrev_b32 v75, 1, v75\n\t"
      "v_lshl_or_b32 v75, v76, 3, v75\n\t"
      "v_and_b32 v76, 8, v0\n\t"
      "v_lshlrev_b32 v76, 3, v76\n\t"
      "v_xor_b32 v75, v75, v76\n\t"
      "v_bfe_u32 v76, v0, 5, 1\n\t"
      "v_lshl_add_u32 v76, v76, 2, v73\n\t"
      "v_and_b32 v77, 1, v0\n\t"
      "v_lshlrev_b32 v77, 3, v77\n\t"
      "v_lshl_or_b32 v76, v76, 7, v77\n\t"
      "s_lshl_b32 s56, s47, 6\n\t"
      "s_lshl_b32 s57, s46, 10\n\t"
      "v_xad_u32 v77, v75, s56, v76\n\t"
      "v_add_u32 v35, s57, v77\n\t"
      "s_lshl_b32 s56, s46, 6\n\t"
      "s_lshl_b32 s57, s47, 12\n\t"
      "s_add_u32 s57, s57, 0x24800\n\t"
      "v_xad_u32 v77, v75, s56, v76\n\t"
      "v_add_u32 v37, s57, v77\n\t"
      "s_mul_i32 s12, s44, 8704\n\t"
      "s_add_u32 s12, s12, 0x100000\n\t"
      "s_add_u32 s13, s12, 4352\n\t"
      "s_mul_i32 s56, s46, 4352\n\t"
      "s_lshl_b32 s57, s47, 11\n\t"
      "s_add_u32 s14, s12, s56\n\t"
      "s_add_u32 s14, s14, s57\n\t"
      "s_add_u32 s81, s14, 0x300000\n\t"
      "s_movk_i32 s20, 4096\n\t"
      "s_movk_i32 s21, 8192\n\t"
      "s_movk_i32 s22, 12288\n\t"
      "s_movk_i32 s23, 16384\n\t"
      "s_movk_i32 s24, 24576\n\t"
      "v_lshlrev_b32 v255, 3, v0\n\t"
      "v_mov_b32 v246, 304\n\t"
      "v_mov_b32 v72, 0\n\t"
      "ds_write_b32 v246, v72\n\t"
      "s_mul_i32 s57, s45, 7184\n\t"
      "s_add_u32 s15, s57, 44544\n\t"
      "s_cmp_eq_u32 s45, 3\n\t"
      "s_cselect_b32 s15, 0x260c0, s15\n\t"
      "s_add_u32 s41, s57, 0x11000\n\t"
      "s_mul_i32 s56, s45, 0x1c000\n\t"
      "s_add_u32 s76, s76, s56\n\t"
      "s_add_u32 s16, s76, 0x150000\n\t"
      "s_add_u32 s76, s76, 0x0\n\t"
      "v_add_u32 v252, 0xf00000, v50\n\t"
      "v_add_u32 v253, 0xf00000, v21\n\t"
      "v_mov_b32 v248, -1\n\t"
      "v_mov_b32 v249, -1\n\t"
      "v_mov_b32 v250, -1\n\t"
      "v_mov_b32 v251, -1\n\t"
      "s_cmp_eq_u32 s45, 3\n\t"
      "s_cselect_b32 s56, 0, 0x400\n\t"
      "v_add_u32 v254, s56, v30\n\t"
      "s_lshl_b32 s72, s50, 1\n\t"
      "s_add_u32 s72, s72, s46\n\t"
      "s_mul_i32 s56, s49, 7\n\t"
      "s_lshl_b32 s57, s45, 1\n\t"
      "s_add_u32 s56, s56, s57\n\t"
      "s_mul_i32 s57, s48, 448\n\t"
      "s_add_u32 s56, s56, s57\n\t"
      "s_lshl_b32 s56, s56, 10\n\t"
      "s_add_u32 s56, s56, 0x800000\n\t"
      "s_xor_b32 s57, s72, 0\n\t"
      "s_mul_i32 s57, s57, 0x1c000\n\t"
      "s_add_u32 s77, s56, s57\n\t"
      "s_xor_b32 s57, s72, 1\n\t"
      "s_mul_i32 s57, s57, 0x1c000\n\t"
      "s_add_u32 s78, s56, s57\n\t"
      "s_xor_b32 s57, s72, 2\n\t"
      "s_mul_i32 s57, s57, 0x1c000\n\t"
      "s_add_u32 s79, s56, s57\n\t"
      "s_xor_b32 s57, s72, 3\n\t"
      "s_mul_i32 s57, s57, 0x1c000\n\t"
      "s_add_u32 s80, s56, s57\n\t"
      "v_bfe_u32 v72, v0, 2, 2\n\t"
      "v_bfe_u32 v73, v0, 5, 1\n\t"
      "v_lshl_add_u32 v73, v73, 2, v72\n\t"
      "v_and_b32 v74, 3, v0\n\t"
      "v_and_b32 v75, 16, v0\n\t"
      "v_lshlrev_b32 v74, 3, v74\n\t"
      "v_lshl_or_b32 v74, v75, 1, v74\n\t"
      "v_lshl_or_b32 v73, v73, 9, v74\n\t"
      "v_xor_b32 v72, s72, v72\n\t"
      "v_lshlrev_b32 v72, 6, v72\n\t"
      "s_lshl_b32 s56, s47, 12\n\t"
      "s_add_u32 s57, s56, 2048\n\t"
      "s_xor_b32 s56, s56, 0x1000\n\t"
      "s_add_u32 s56, s56, 2048\n\t"
      "v_add_u32 v74, s57, v73\n\t"
      "v_add_u32 v75, s56, v73\n\t"
      "s_movk_i32 s58, 0x80\n\t"
      "s_movk_i32 s59, 0xc0\n\t"
      "v_xad_u32 v188, v72, 0, v74\n\t"
      "v_xad_u32 v189, v72, 64, v74\n\t"
      "v_xad_u32 v190, v72, s58, v74\n\t"
      "v_xad_u32 v191, v72, s59, v74\n\t"
      "v_xad_u32 v192, v72, 0, v75\n\t"
      "v_xad_u32 v193, v72, 64, v75\n\t"
      "v_xad_u32 v194, v72, s58, v75\n\t"
      "v_xad_u32 v195, v72, s59, v75\n\t"
      "v_bfe_u32 v72, v0, 5, 1\n\t"
      "v_lshlrev_b32 v72, 2, v72\n\t"
      "v_xor_b32 v72, v72, v213\n\t"
      "v_lshlrev_b32 v72, 4, v72\n\t"
      "s_movk_i32 s59, 4352\n\t"
      "v_mul_u32_u24 v73, s59, v212\n\t"
      "v_add_u32 v196, v72, v73\n\t"
      "v_xad_u32 v74, v72, s58, v73\n\t"
      "v_add_u32 v197, 0x4000, v74\n\t"
      "v_add_u32 v198, 0x8000, v196\n\t"
      "v_add_u32 v199, 0xc000, v74\n\t"
      "s_lshl_b32 s56, s48, 8\n\t"
      "s_add_u32 s56, s56, 0x400000\n\t"
      "s_mul_i32 s57, s45, 69632\n\t"
      "s_add_u32 s82, s56, s57\n\t"
      "s_waitcnt lgkmcnt(0)\n\t"
      "s_lshl_b32 s56, s40, 16\n\t"
      "s_and_b32 s57, s40, 0xffff0000\n\t"
      "s_lshl_b32 s58, s42, 16\n\t"
      "s_and_b32 s42, s42, 0xffff0000\n\t"
      "s_cmp_eq_u32 s46, 0\n\t"
      "s_cselect_b32 s56, s56, s57\n\t"
      "s_cselect_b32 s42, s58, s42\n\t"
      "v_mov_b32 v76, 0x3fb8aa3b\n\t"
      "v_mul_f32 v76, s56, v76\n\t"
      "v_exp_f32 v76, v76\n\t"
      "s_nop 0\n\t"
      "v_cvt_pk_bf16_f32 v76, v76, v76\n\t"
      "v_lshlrev_b32 v49, 16, v76\n\t"
      "s_cmp_eq_u32 s47, 0\n\t"
      "s_cselect_b32 s42, s42, 0\n\t"
      "s_mov_b32 m0, s87\n\t"
      "s_cmp_gt_u32 s45, 1\n\t"
      "s_cbranch_scc1 Lp1b%=\n\t"
      "s_waitcnt vmcnt(4)\n\t"
      "v_add_u32 v128, 1, v128\n\t"
      "v_mov_b32 v76, 0\n\t"
      "ds_write_b32 v76, v128 offset:512\n\t"
      "v_lshl_or_b32 v77, v128, 1, 1\n\t"
      "v_mfma_f32_16x16x32_bf16 v[136:139], v[96:99], v[52:55], 0\n\t"
      "v_perm_b32 v112, v82, v80, s74\n\t"
      "v_perm_b32 v113, v86, v84, s74\n\t"
      "v_mfma_f32_16x16x32_bf16 v[140:143], v[96:99], v[96:99], 0\n\t"
      "v_perm_b32 v114, v90, v88, s74\n\t"
      "v_perm_b32 v115, v94, v92, s74\n\t"
      "buffer_store_dwordx4 v[112:115], v50, s[8:11], s68 offen sc1\n\t"
      "v_mfma_f32_16x16x32_bf16 v[136:139], v[100:103], v[52:55], v[136:139]\n\t"
      "v_perm_b32 v116, v82, v80, s75\n\t"
      "v_perm_b32 v117, v86, v84, s75\n\t"
      "v_mfma_f32_16x16x32_bf16 v[140:143], v[100:103], v[100:103], v[140:143]\n\t"
      "v_perm_b32 v118, v90, v88, s75\n\t"
      "v_perm_b32 v119, v94, v92, s75\n\t"
      "buffer_store_dwordx4 v[116:119], v50, s[8:11], s69 offen sc1\n\t"
      "v_mfma_f32_16x16x32_bf16 v[136:139], v[104:107], v[52:55], v[136:139]\n\t"
      "v_perm_b32 v120, v83, v81, s74\n\t"
      "v_perm_b32 v121, v87, v85, s74\n\t"
      "v_mfma_f32_16x16x32_bf16 v[140:143], v[104:107], v[104:107], v[140:143]\n\t"
      "v_perm_b32 v122, v91, v89, s74\n\t"
      "v_perm_b32 v123, v95, v93, s74\n\t"
      "buffer_store_dwordx4 v[120:123], v50, s[8:11], s70 offen sc1\n\t"
      "v_mfma_f32_16x16x32_bf16 v[136:139], v[108:111], v[52:55], v[136:139]\n\t"
      "v_perm_b32 v124, v83, v81, s75\n\t"
      "v_perm_b32 v125, v87, v85, s75\n\t"
      "v_mfma_f32_16x16x32_bf16 v[140:143], v[108:111], v[108:111], v[140:143]\n\t"
      "v_perm_b32 v126, v91, v89, s75\n\t"
      "v_perm_b32 v127, v95, v93, s75\n\t"
      "buffer_store_dwordx4 v[124:127], v50, s[8:11], s71 offen sc1\n\t"
      "s_nop 1\n\t"
      "v_cndmask_b32 v136, v136, v137, s[60:61]\n\t"
      "v_cndmask_b32 v138, v138, v139, s[60:61]\n\t"
      "s_nop 0\n\t"
      "v_cndmask_b32 v140, v140, v141, s[60:61]\n\t"
      "v_cndmask_b32 v142, v142, v143, s[60:61]\n\t"
      "v_cndmask_b32 v136, v136, v138, s[62:63]\n\t"
      "v_cndmask_b32 v137, v140, v142, s[62:63]\n\t"
      "s_mov_b64 exec, s[64:65]\n\t"
      "ds_write_b64 v46, v[136:137]\n\t"
      "s_mov_b64 exec, -1\n\t"
      "v_xor_b32 v144, v77, v112\n\t"
      "v_xor_b32 v145, v77, v113\n\t"
      "v_xor_b32 v146, v77, v114\n\t"
      "v_xor_b32 v147, v77, v115\n\t"
      "buffer_store_dwordx4 v[144:147], v252, s[8:11], s68 offen sc1\n\t"
      "v_xor_b32 v148, v77, v116\n\t"
      "v_xor_b32 v149, v77, v117\n\t"
      "v_xor_b32 v150, v77, v118\n\t"
      "v_xor_b32 v151, v77, v119\n\t"
      "buffer_store_dwordx4 v[148:151], v252, s[8:11], s69 offen sc1\n\t"
      "v_xor_b32 v152, v77, v120\n\t"
      "v_xor_b32 v153, v77, v121\n\t"
      "v_xor_b32 v154, v77, v122\n\t"
      "v_xor_b32 v155, v77, v123\n\t"
      "buffer_store_dwordx4 v[152:155], v252, s[8:11], s70 offen sc1\n\t"
      "v_xor_b32 v156, v77, v124\n\t"
      "v_xor_b32 v157, v77, v125\n\t"
      "v_xor_b32 v158, v77, v126\n\t"
      "v_xor_b32 v159, v77, v127\n\t"
      "buffer_store_dwordx4 v[156:159], v252, s[8:11], s71 offen sc1\n\t"
      "v_lshlrev_b32 v71, 7, v1\n\t"
      "s_mul_i32 s56, s44, 7168\n\t"
      "s_mul_i32 s57, s47, 3584\n\t"
      "s_add_u32 s56, s56, s57\n\t"
      "s_mov_b64 exec, 0xfffffff\n\t"
      "buffer_load_dword v63, v71, s[32:35], s56 offen\n\t"
      "s_mov_b64 exec, -1\n\t"
      "s_waitcnt vmcnt(9)\n\t"
      "s_branch Lp1d%=\n\t"
      "Lp1b%=:\n\t"
      "s_waitcnt vmcnt(2)\n\t"
      "Lp1d%=:\n\t"
      "v_lshlrev_b32 v64, 16, v129\n\t"
      "v_lshlrev_b32 v65, 16, v130\n\t"
      "v_mul_f32 v64, 0x3fb8aa3b, v64\n\t"
      "v_lshlrev_b32 v66, 16, v131\n\t"
      "v_exp_f32 v64, v64\n\t"
      "v_lshlrev_b32 v67, 16, v132\n\t"
      "v_mul_f32 v65, v65, v49\n\t"
      "v_mul_f32 v66, v66, v49\n\t"
      "v_cvt_pk_bf16_f32 v64, v64, v64\n\t"
      "v_mul_f32 v67, v67, v49\n\t"
      "v_lshlrev_b32 v64, 16, v64\n\t"
      "v_mul_f32 v65, 0.15915494, v65\n\t"
      "v_mul_f32 v64, v64, v49\n\t"
      "v_add_f32 v66, v66, v66\n\t"
      "v_mul_f32 v64, 0xbfb8aa3b, v64\n\t"
      "v_mul_f32 v67, -2.0, v67\n\t"
      "v_mul_f32 v80, 0x41000000, v64\n\t"
      "v_mul_f32 v81, 0xc1000000, v65\n\t"
      "v_exp_f32 v80, v80\n\t"
      "v_fract_f32 v81, v81\n\t"
      "v_exp_f32 v84, v64\n\t"
      "v_cos_f32 v82, v81\n\t"
      "v_sin_f32 v83, v81\n\t"
      "v_fract_f32 v85, v65\n\t"
      "v_pk_mul_f32 v[82:83], v[82:83], v[80:81] op_sel_hi:[1,0]\n\t"
      "v_cos_f32 v86, v85\n\t"
      "v_sin_f32 v87, v85\n\t"
      "v_mul_f32 v88, 0x43800000, v64\n\t"
      "v_pk_mul_f32 v[86:87], v[86:87], v[84:85] op_sel_hi:[1,0]\n\t"
      "v_mul_f32 v89, 0x43800000, v65\n\t"
      "s_waitcnt lgkmcnt(0)\n\t"
      "s_barrier\n\t"
      "v_mov_b32 v177, 0\n\t"
      "ds_read_b32 v177, v177 offset:512\n\t"
      "ds_read_b128 v[180:183], v47\n\t"
      "ds_read_b128 v[184:187], v47 offset:16\n\t"
      "v_exp_f32 v88, v88\n\t"
      "v_fract_f32 v89, v89\n\t"
      "v_pk_mul_f32 v[92:93], v[82:83], v[82:83] op_sel_hi:[0,1]\n\t"
      "v_cos_f32 v90, v89\n\t"
      "v_sin_f32 v91, v89\n\t"
      "s_waitcnt lgkmcnt(0)\n\t"
      "v_pk_fma_f32 v[92:93], v[82:83], v[82:83], v[92:93] op_sel:[1,1,0] op_sel_hi:[1,0,1] neg_lo:[1,0,0]\n\t"
      "v_add_f32 v180, v180, v182\n\t"
      "v_pk_mul_f32 v[90:91], v[90:91], v[88:89] op_sel_hi:[1,0]\n\t"
      "v_add_f32 v184, v184, v186\n\t"
      "v_pk_mul_f32 v[94:95], v[66:67], v[82:83] op_sel_hi:[0,1]\n\t"
      "v_add_f32 v181, v181, v183\n\t"
      "v_pk_mul_f32 v[96:97], v[86:87], v[86:87] op_sel_hi:[0,1]\n\t"
      "v_add_f32 v185, v185, v187\n\t"
      "v_pk_fma_f32 v[94:95], v[66:67], v[82:83], v[94:95] op_sel:[1,1,0] op_sel_hi:[1,0,1] neg_lo:[1,0,0]\n\t"
      "v_add_f32 v180, v180, v184\n\t"
      "v_pk_fma_f32 v[96:97], v[86:87], v[86:87], v[96:97] op_sel:[1,1,0] op_sel_hi:[1,0,1] neg_lo:[1,0,0]\n\t"
      "v_add_f32 v181, v181, v185\n\t"
      "v_pk_mul_f32 v[98:99], v[90:91], v[90:91] op_sel_hi:[0,1]\n\t"
      "v_mul_f32 v180, 0x3b000000, v180\n\t"
      "v_pk_mul_f32 v[100:101], v[92:93], v[92:93] op_sel_hi:[0,1]\n\t"
      "v_mul_f32 v181, 0x3b000000, v181\n\t"
      "v_pk_fma_f32 v[98:99], v[90:91], v[90:91], v[98:99] op_sel:[1,1,0] op_sel_hi:[1,0,1] neg_lo:[1,0,0]\n\t"
      "v_fma_f32 v181, -v180, v180, v181\n\t"
      "v_pk_fma_f32 v[100:101], v[92:93], v[92:93], v[100:101] op_sel:[1,1,0] op_sel_hi:[1,0,1] neg_lo:[1,0,0]\n\t"
      "v_max_f32 v181, 0, v181\n\t"
      "v_pk_mul_f32 v[102:103], v[66:67], v[92:93] op_sel_hi:[0,1]\n\t"
      "v_cvt_pk_bf16_f32 v181, v181, v181\n\t"
      "v_pk_mul_f32 v[104:105], v[94:95], v[92:93] op_sel_hi:[0,1]\n\t"
      "v_lshlrev_b32 v181, 16, v181\n\t"
      "v_pk_fma_f32 v[102:103], v[66:67], v[92:93], v[102:103] op_sel:[1,1,0] op_sel_hi:[1,0,1] neg_lo:[1,0,0]\n\t"
      "v_add_f32 v181, 0x3727c5ac, v181\n\t"
      "v_pk_fma_f32 v[104:105], v[94:95], v[92:93], v[104:105] op_sel:[1,1,0] op_sel_hi:[1,0,1] neg_lo:[1,0,0]\n\t"
      "v_cvt_pk_bf16_f32 v181, v181, v181\n\t"
      "v_mov_b32 v68, 0x3f80\n\t"
      "v_lshlrev_b32 v181, 16, v181\n\t"
      "v_cvt_pk_bf16_f32 v69, v66, v67\n\t"
      "v_sqrt_f32 v181, v181\n\t"
      "v_cvt_pk_bf16_f32 v70, v86, v87\n\t"
      "v_readfirstlane_b32 s53, v177\n\t"
      "v_cvt_pk_bf16_f32 v71, v90, v91\n\t"
      "v_cvt_pk_bf16_f32 v176, v180, v181\n\t"
      "s_lshl_b32 s56, s44, 4\n\t"
      "s_add_u32 s56, s56, 4096\n\t"
      "s_cmp_eq_u32 s45, 0\n\t"
      "s_cselect_b64 exec, 0xff, 0\n\t"
      "buffer_store_dwordx2 v[176:177], v48, s[8:11], s56 offen sc1\n\t"
      "s_mov_b64 exec, -1\n\t"
      "s_lshl_b32 s18, s53, 1\n\t"
      "s_or_b32 s18, s18, 1\n\t"
      "v_mov_b32 v242, v177\n\t"
      "v_cvt_pk_bf16_f32 v72, v96, v97\n\t"
      "buffer_store_dwordx4 v[248:251], v30, s[8:11], s81 offen sc1\n\t"
      "v_cvt_pk_bf16_f32 v73, v98, v99\n\t"
      "ds_write_addtid_b32 v68 offset:17408\n\t"
      "ds_write_addtid_b32 v69 offset:0\n\t"
      "ds_write_addtid_b32 v70 offset:17952\n\t"
      "ds_write_addtid_b32 v71 offset:21760\n\t"
      "ds_write_addtid_b32 v72 offset:18496\n\t"
      "ds_write_addtid_b32 v73 offset:23936\n\t"
      "v_cvt_pk_bf16_f32 v74, v94, v95\n\t"
      "v_cvt_pk_bf16_f32 v75, v102, v103\n\t"
      "v_cvt_pk_bf16_f32 v76, v104, v105\n\t"
      "v_pk_mul_f32 v[122:123], v[96:97], v[86:87] op_sel_hi:[0,1]\n\t"
      "v_pk_mul_f32 v[124:125], v[96:97], v[96:97] op_sel_hi:[0,1]\n\t"
      "v_pk_fma_f32 v[122:123], v[96:97], v[86:87], v[122:123] op_sel:[1,1,0] op_sel_hi:[1,0,1] neg_lo:[1,0,0]\n\t"
      "ds_write_addtid_b32 v74 offset:544\n\t"
      "ds_write_addtid_b32 v75 offset:1088\n\t"
      "ds_write_addtid_b32 v76 offset:1632\n\t"
      "v_cvt_pk_bf16_f32 v77, v122, v123\n\t"
      "v_pk_fma_f32 v[124:125], v[96:97], v[96:97], v[124:125] op_sel:[1,1,0] op_sel_hi:[1,0,1] neg_lo:[1,0,0]\n\t"
      "v_pk_mul_f32 v[108:109], v[66:67], v[100:101] op_sel_hi:[0,1]\n\t"
      "v_cvt_pk_bf16_f32 v78, v124, v125\n\t"
      "v_pk_fma_f32 v[108:109], v[66:67], v[100:101], v[108:109] op_sel:[1,1,0] op_sel_hi:[1,0,1] neg_lo:[1,0,0]\n\t"
      "v_pk_mul_f32 v[112:113], v[94:95], v[100:101] op_sel_hi:[0,1]\n\t"
      "ds_write_addtid_b32 v77 offset:19040\n\t"
      "v_cvt_pk_bf16_f32 v79, v108, v109\n\t"
      "buffer_store_dwordx4 v[248:251], v30, s[8:11], s81 offen offset:1024 sc1\n\t"
      "v_pk_fma_f32 v[112:113], v[94:95], v[100:101], v[112:113] op_sel:[1,1,0] op_sel_hi:[1,0,1] neg_lo:[1,0,0]\n\t"
      "ds_write_addtid_b32 v78 offset:19584\n\t"
      "v_cvt_pk_bf16_f32 v68, v112, v113\n\t"
      "v_pk_mul_f32 v[116:117], v[102:103], v[100:101] op_sel_hi:[0,1]\n\t"
      "v_pk_mul_f32 v[120:121], v[104:105], v[100:101] op_sel_hi:[0,1]\n\t"
      "ds_write_addtid_b32 v79 offset:2176\n\t"
      "v_pk_fma_f32 v[116:117], v[102:103], v[100:101], v[116:117] op_sel:[1,1,0] op_sel_hi:[1,0,1] neg_lo:[1,0,0]\n\t"
      "v_pk_fma_f32 v[120:121], v[104:105], v[100:101], v[120:121] op_sel:[1,1,0] op_sel_hi:[1,0,1] neg_lo:[1,0,0]\n\t"
      "ds_write_addtid_b32 v68 offset:2720\n\t"
      "v_cvt_pk_bf16_f32 v69, v116, v117\n\t"
      "v_cvt_pk_bf16_f32 v70, v120, v121\n\t"
      "v_pk_mul_f32 v[126:127], v[98:99], v[90:91] op_sel_hi:[0,1]\n\t"
      "v_pk_mul_f32 v[128:129], v[98:99], v[98:99] op_sel_hi:[0,1]\n\t"
      "v_pk_fma_f32 v[126:127], v[98:99], v[90:91], v[126:127] op_sel:[1,1,0] op_sel_hi:[1,0,1] neg_lo:[1,0,0]\n\t"
      "v_pk_fma_f32 v[128:129], v[98:99], v[98:99], v[128:129] op_sel:[1,1,0] op_sel_hi:[1,0,1] neg_lo:[1,0,0]\n\t"
      "ds_write_addtid_b32 v69 offset:3264\n\t"
      "ds_write_addtid_b32 v70 offset:3808\n\t"
      "v_cvt_pk_bf16_f32 v71, v126, v127\n\t"
      "v_cvt_pk_bf16_f32 v72, v128, v129\n\t"
      "v_pk_mul_f32 v[106:107], v[108:109], v[100:101] op_sel_hi:[0,1]\n\t"
      "v_pk_mul_f32 v[110:111], v[112:113], v[100:101] op_sel_hi:[0,1]\n\t"
      "v_pk_fma_f32 v[106:107], v[108:109], v[100:101], v[106:107] op_sel:[1,1,0] op_sel_hi:[1,0,1] neg_lo:[1,0,0]\n\t"
      "v_pk_fma_f32 v[110:111], v[112:113], v[100:101], v[110:111] op_sel:[1,1,0] op_sel_hi:[1,0,1] neg_lo:[1,0,0]\n\t"
      "ds_write_addtid_b32 v71 offset:25024\n\t"
      "s_add_u32 m0, s41, 0x0\n\t"
      "s_nop 0\n\t"
      "buffer_load_dwordx4 v56, s[28:31], s76 offen lds\n\t"
      "s_mov_b32 m0, s87\n\t"
      "s_nop 0\n\t"
      "ds_write_addtid_b32 v72 offset:26112\n\t"
      "v_cvt_pk_bf16_f32 v73, v106, v107\n\t"
      "v_cvt_pk_bf16_f32 v74, v110, v111\n\t"
      "v_pk_mul_f32 v[114:115], v[116:117], v[100:101] op_sel_hi:[0,1]\n\t"
      "v_pk_mul_f32 v[118:119], v[120:121], v[100:101] op_sel_hi:[0,1]\n\t"
      "v_pk_fma_f32 v[114:115], v[116:117], v[100:101], v[114:115] op_sel:[1,1,0] op_sel_hi:[1,0,1] neg_lo:[1,0,0]\n\t"
      "v_pk_fma_f32 v[118:119], v[120:121], v[100:101], v[118:119] op_sel:[1,1,0] op_sel_hi:[1,0,1] neg_lo:[1,0,0]\n\t"
      "ds_write_addtid_b32 v73 offset:4352\n\t"
      "ds_write_addtid_b32 v74 offset:4896\n\t"
      "v_cvt_pk_bf16_f32 v75, v114, v115\n\t"
      "v_cvt_pk_bf16_f32 v76, v118, v119\n\t"
      "v_pk_mul_f32 v[130:131], v[124:125], v[86:87] op_sel_hi:[0,1]\n\t"
      "v_pk_mul_f32 v[132:133], v[122:123], v[122:123] op_sel_hi:[0,1]\n\t"
      "v_pk_fma_f32 v[130:131], v[124:125], v[86:87], v[130:131] op_sel:[1,1,0] op_sel_hi:[1,0,1] neg_lo:[1,0,0]\n\t"
      "v_pk_fma_f32 v[132:133], v[122:123], v[122:123], v[132:133] op_sel:[1,1,0] op_sel_hi:[1,0,1] neg_lo:[1,0,0]\n\t"
      "ds_write_addtid_b32 v75 offset:5440\n\t"
      "ds_write_addtid_b32 v76 offset:5984\n\t"
      "v_cvt_pk_bf16_f32 v77, v130, v131\n\t"
      "v_cvt_pk_bf16_f32 v78, v132, v133\n\t"
      "v_pk_mul_f32 v[108:109], v[106:107], v[100:101] op_sel_hi:[0,1]\n\t"
      "v_pk_mul_f32 v[112:113], v[110:111], v[100:101] op_sel_hi:[0,1]\n\t"
      "v_pk_fma_f32 v[108:109], v[106:107], v[100:101], v[108:109] op_sel:[1,1,0] op_sel_hi:[1,0,1] neg_lo:[1,0,0]\n\t"
      "v_pk_fma_f32 v[112:113], v[110:111], v[100:101], v[112:113] op_sel:[1,1,0] op_sel_hi:[1,0,1] neg_lo:[1,0,0]\n\t"
      "ds_write_addtid_b32 v77 offset:20128\n\t"
      "s_add_u32 m0, s41, 0x0\n\t"
      "s_nop 0\n\t"
      "buffer_load_dwordx4 v57, s[28:31], s76 offen offset:1024 lds\n\t"
      "s_mov_b32 m0, s87\n\t"
      "s_nop 0\n\t"
      "ds_write_addtid_b32 v78 offset:20672\n\t"
      "v_cvt_pk_bf16_f32 v79, v108, v109\n\t"
      "v_cvt_pk_bf16_f32 v68, v112, v113\n\t"
      "v_pk_mul_f32 v[116:117], v[114:115], v[100:101] op_sel_hi:[0,1]\n\t"
      "v_pk_mul_f32 v[120:121], v[118:119], v[100:101] op_sel_hi:[0,1]\n\t"
      "v_pk_fma_f32 v[116:117], v[114:115], v[100:101], v[116:117] op_sel:[1,1,0] op_sel_hi:[1,0,1] neg_lo:[1,0,0]\n\t"
      "v_pk_fma_f32 v[120:121], v[118:119], v[100:101], v[120:121] op_sel:[1,1,0] op_sel_hi:[1,0,1] neg_lo:[1,0,0]\n\t"
      "ds_write_addtid_b32 v79 offset:6528\n\t"
      "ds_write_addtid_b32 v68 offset:7072\n\t"
      "v_cvt_pk_bf16_f32 v69, v116, v117\n\t"
      "v_cvt_pk_bf16_f32 v70, v120, v121\n\t"
      "v_pk_mul_f32 v[134:135], v[124:125], v[122:123] op_sel_hi:[0,1]\n\t"
      "v_pk_mul_f32 v[136:137], v[90:91], v[96:97] op_sel_hi:[0,1]\n\t"
      "v_pk_fma_f32 v[134:135], v[124:125], v[122:123], v[134:135] op_sel:[1,1,0] op_sel_hi:[1,0,1] neg_lo:[1,0,0]\n\t"
      "v_pk_fma_f32 v[136:137], v[90:91], v[96:97], v[136:137] op_sel:[1,1,0] op_sel_hi:[1,0,1] neg_lo:[1,0,0]\n\t"
      "ds_write_addtid_b32 v69 offset:7616\n\t"
      "ds_write_addtid_b32 v70 offset:8160\n\t"
      "v_cvt_pk_bf16_f32 v71, v134, v135\n\t"
      "v_cvt_pk_bf16_f32 v72, v136, v137\n\t"
      "v_pk_mul_f32 v[106:107], v[108:109], v[100:101] op_sel_hi:[0,1]\n\t"
      "v_pk_mul_f32 v[110:111], v[112:113], v[100:101] op_sel_hi:[0,1]\n\t"
      "v_pk_fma_f32 v[106:107], v[108:109], v[100:101], v[106:107] op_sel:[1,1,0] op_sel_hi:[1,0,1] neg_lo:[1,0,0]\n\t"
      "v_pk_fma_f32 v[110:111], v[112:113], v[100:101], v[110:111] op_sel:[1,1,0] op_sel_hi:[1,0,1] neg_lo:[1,0,0]\n\t"
      "ds_write_addtid_b32 v71 offset:21216\n\t"
      "s_add_u32 m0, s41, 0x0\n\t"
      "s_nop 0\n\t"
      "buffer_load_dwordx4 v58, s[28:31], s76 offen offset:2048 lds\n\t"
      "s_mov_b32 m0, s87\n\t"
      "s_nop 0\n\t"
      "ds_write_addtid_b32 v72 offset:22304\n\t"
      "v_cvt_pk_bf16_f32 v73, v106, v107\n\t"
      "v_cvt_pk_bf16_f32 v74, v110, v111\n\t"
      "v_pk_mul_f32 v[114:115], v[116:117], v[100:101] op_sel_hi:[0,1]\n\t"
      "v_pk_mul_f32 v[118:119], v[120:121], v[100:101] op_sel_hi:[0,1]\n\t"
      "v_pk_fma_f32 v[114:115], v[116:117], v[100:101], v[114:115] op_sel:[1,1,0] op_sel_hi:[1,0,1] neg_lo:[1,0,0]\n\t"
      "v_pk_fma_f32 v[118:119], v[120:121], v[100:101], v[118:119] op_sel:[1,1,0] op_sel_hi:[1,0,1] neg_lo:[1,0,0]\n\t"
      "ds_write_addtid_b32 v73 offset:8704\n\t"
      "ds_write_addtid_b32 v74 offset:9248\n\t"
      "v_cvt_pk_bf16_f32 v75, v114, v115\n\t"
      "v_cvt_pk_bf16_f32 v76, v118, v119\n\t"
      "v_pk_mul_f32 v[138:139], v[90:91], v[124:125] op_sel_hi:[0,1]\n\t"
      "v_pk_mul_f32 v[140:141], v[90:91], v[132:133] op_sel_hi:[0,1]\n\t"
      "v_pk_fma_f32 v[138:139], v[90:91], v[124:125], v[138:139] op_sel:[1,1,0] op_sel_hi:[1,0,1] neg_lo:[1,0,0]\n\t"
      "v_pk_fma_f32 v[140:141], v[90:91], v[132:133], v[140:141] op_sel:[1,1,0] op_sel_hi:[1,0,1] neg_lo:[1,0,0]\n\t"
      "ds_write_addtid_b32 v75 offset:9792\n\t"
      "ds_write_addtid_b32 v76 offset:10336\n\t"
      "v_cvt_pk_bf16_f32 v77, v138, v139\n\t"
      "v_cvt_pk_bf16_f32 v78, v140, v141\n\t"
      "v_pk_mul_f32 v[108:109], v[106:107], v[100:101] op_sel_hi:[0,1]\n\t"
      "v_pk_mul_f32 v[112:113], v[110:111], v[100:101] op_sel_hi:[0,1]\n\t"
      "v_pk_fma_f32 v[108:109], v[106:107], v[100:101], v[108:109] op_sel:[1,1,0] op_sel_hi:[1,0,1] neg_lo:[1,0,0]\n\t"
      "v_pk_fma_f32 v[112:113], v[110:111], v[100:101], v[112:113] op_sel:[1,1,0] op_sel_hi:[1,0,1] neg_lo:[1,0,0]\n\t"
      "ds_write_addtid_b32 v77 offset:22848\n\t"
      "s_add_u32 m0, s41, 0x0\n\t"
      "s_nop 0\n\t"
      "buffer_load_dwordx4 v59, s[28:31], s76 offen offset:3072 lds\n\t"
      "s_mov_b32 m0, s87\n\t"
      "s_nop 0\n\t"
      "ds_write_addtid_b32 v78 offset:23392\n\t"
      "v_cvt_pk_bf16_f32 v79, v108, v109\n\t"
      "v_cvt_pk_bf16_f32 v68, v112, v113\n\t"
      "v_pk_mul_f32 v[116:117], v[114:115], v[100:101] op_sel_hi:[0,1]\n\t"
      "v_pk_mul_f32 v[120:121], v[118:119], v[100:101] op_sel_hi:[0,1]\n\t"
      "v_pk_fma_f32 v[116:117], v[114:115], v[100:101], v[116:117] op_sel:[1,1,0] op_sel_hi:[1,0,1] neg_lo:[1,0,0]\n\t"
      "v_pk_fma_f32 v[120:121], v[118:119], v[100:101], v[120:121] op_sel:[1,1,0] op_sel_hi:[1,0,1] neg_lo:[1,0,0]\n\t"
      "ds_write_addtid_b32 v79 offset:10880\n\t"
      "ds_write_addtid_b32 v68 offset:11424\n\t"
      "v_cvt_pk_bf16_f32 v69, v116, v117\n\t"
      "v_cvt_pk_bf16_f32 v70, v120, v121\n\t"
      "v_pk_mul_f32 v[142:143], v[98:99], v[124:125] op_sel_hi:[0,1]\n\t"
      "v_pk_mul_f32 v[144:145], v[126:127], v[124:125] op_sel_hi:[0,1]\n\t"
      "v_pk_fma_f32 v[142:143], v[98:99], v[124:125], v[142:143] op_sel:[1,1,0] op_sel_hi:[1,0,1] neg_lo:[1,0,0]\n\t"
      "v_pk_fma_f32 v[144:145], v[126:127], v[124:125], v[144:145] op_sel:[1,1,0] op_sel_hi:[1,0,1] neg_lo:[1,0,0]\n\t"
      "ds_write_addtid_b32 v69 offset:11968\n\t"
      "ds_write_addtid_b32 v70 offset:12512\n\t"
      "v_cvt_pk_bf16_f32 v71, v142, v143\n\t"
      "v_cvt_pk_bf16_f32 v72, v144, v145\n\t"
      "v_pk_mul_f32 v[106:107], v[108:109], v[100:101] op_sel_hi:[0,1]\n\t"
      "v_pk_mul_f32 v[110:111], v[112:113], v[100:101] op_sel_hi:[0,1]\n\t"
      "v_pk_fma_f32 v[106:107], v[108:109], v[100:101], v[106:107] op_sel:[1,1,0] op_sel_hi:[1,0,1] neg_lo:[1,0,0]\n\t"
      "v_pk_fma_f32 v[110:111], v[112:113], v[100:101], v[110:111] op_sel:[1,1,0] op_sel_hi:[1,0,1] neg_lo:[1,0,0]\n\t"
      "ds_write_addtid_b32 v71 offset:24480\n\t"
      "s_add_u32 m0, s41, 0x1000\n\t"
      "s_nop 0\n\t"
      "buffer_load_dwordx4 v60, s[28:31], s76 offen lds\n\t"
      "s_mov_b32 m0, s87\n\t"
      "s_nop 0\n\t"
      "ds_write_addtid_b32 v72 offset:25568\n\t"
      "v_cvt_pk_bf16_f32 v73, v106, v107\n\t"
      "v_cvt_pk_bf16_f32 v74, v110, v111\n\t"
      "v_pk_mul_f32 v[114:115], v[116:117], v[100:101] op_sel_hi:[0,1]\n\t"
      "v_pk_mul_f32 v[118:119], v[120:121], v[100:101] op_sel_hi:[0,1]\n\t"
      "v_pk_fma_f32 v[114:115], v[116:117], v[100:101], v[114:115] op_sel:[1,1,0] op_sel_hi:[1,0,1] neg_lo:[1,0,0]\n\t"
      "v_pk_fma_f32 v[118:119], v[120:121], v[100:101], v[118:119] op_sel:[1,1,0] op_sel_hi:[1,0,1] neg_lo:[1,0,0]\n\t"
      "ds_write_addtid_b32 v73 offset:13056\n\t"
      "ds_write_addtid_b32 v74 offset:13600\n\t"
      "v_cvt_pk_bf16_f32 v75, v114, v115\n\t"
      "v_cvt_pk_bf16_f32 v76, v118, v119\n\t"
      "v_pk_mul_f32 v[146:147], v[128:129], v[90:91] op_sel_hi:[0,1]\n\t"
      "v_pk_mul_f32 v[148:149], v[128:129], v[98:99] op_sel_hi:[0,1]\n\t"
      "v_pk_fma_f32 v[146:147], v[128:129], v[90:91], v[146:147] op_sel:[1,1,0] op_sel_hi:[1,0,1] neg_lo:[1,0,0]\n\t"
      "v_pk_fma_f32 v[148:149], v[128:129], v[98:99], v[148:149] op_sel:[1,1,0] op_sel_hi:[1,0,1] neg_lo:[1,0,0]\n\t"
      "ds_write_addtid_b32 v75 offset:14144\n\t"
      "ds_write_addtid_b32 v76 offset:14688\n\t"
      "v_cvt_pk_bf16_f32 v77, v146, v147\n\t"
      "v_cvt_pk_bf16_f32 v78, v148, v149\n\t"
      "v_pk_mul_f32 v[108:109], v[106:107], v[100:101] op_sel_hi:[0,1]\n\t"
      "v_pk_mul_f32 v[112:113], v[110:111], v[100:101] op_sel_hi:[0,1]\n\t"
      "v_pk_fma_f32 v[108:109], v[106:107], v[100:101], v[108:109] op_sel:[1,1,0] op_sel_hi:[1,0,1] neg_lo:[1,0,0]\n\t"
      "v_pk_fma_f32 v[112:113], v[110:111], v[100:101], v[112:113] op_sel:[1,1,0] op_sel_hi:[1,0,1] neg_lo:[1,0,0]\n\t"
      "ds_write_addtid_b32 v77 offset:26656\n\t"
      "s_add_u32 m0, s41, 0x1000\n\t"
      "s_nop 0\n\t"
      "buffer_load_dwordx4 v61, s[28:31], s76 offen offset:1024 lds\n\t"
      "s_mov_b32 m0, s87\n\t"
      "s_nop 0\n\t"
      "ds_write_addtid_b32 v78 offset:27200\n\t"
      "v_cvt_pk_bf16_f32 v79, v108, v109\n\t"
      "v_cvt_pk_bf16_f32 v68, v112, v113\n\t"
      "v_pk_mul_f32 v[116:117], v[114:115], v[100:101] op_sel_hi:[0,1]\n\t"
      "v_pk_mul_f32 v[120:121], v[118:119], v[100:101] op_sel_hi:[0,1]\n\t"
      "v_pk_fma_f32 v[116:117], v[114:115], v[100:101], v[116:117] op_sel:[1,1,0] op_sel_hi:[1,0,1] neg_lo:[1,0,0]\n\t"
      "v_pk_fma_f32 v[120:121], v[118:119], v[100:101], v[120:121] op_sel:[1,1,0] op_sel_hi:[1,0,1] neg_lo:[1,0,0]\n\t"
      "ds_write_addtid_b32 v79 offset:15232\n\t"
      "ds_write_addtid_b32 v68 offset:15776\n\t"
      "v_cvt_pk_bf16_f32 v69, v116, v117\n\t"
      "v_cvt_pk_bf16_f32 v70, v120, v121\n\t"
      "v_pk_mul_f32 v[150:151], v[128:129], v[126:127] op_sel_hi:[0,1]\n\t"
      "v_pk_mul_f32 v[152:153], v[128:129], v[128:129] op_sel_hi:[0,1]\n\t"
      "v_pk_fma_f32 v[150:151], v[128:129], v[126:127], v[150:151] op_sel:[1,1,0] op_sel_hi:[1,0,1] neg_lo:[1,0,0]\n\t"
      "v_pk_fma_f32 v[152:153], v[128:129], v[128:129], v[152:153] op_sel:[1,1,0] op_sel_hi:[1,0,1] neg_lo:[1,0,0]\n\t"
      "ds_write_addtid_b32 v69 offset:16320\n\t"
      "ds_write_addtid_b32 v70 offset:16864\n\t"
      "v_cvt_pk_bf16_f32 v71, v150, v151\n\t"
      "v_cvt_pk_bf16_f32 v72, v152, v153\n\t"
      "s_nop 0\n\t"
      "s_nop 0\n\t"
      "s_nop 0\n\t"
      "s_nop 0\n\t"
      "ds_write_addtid_b32 v71 offset:27744\n\t"
      "s_add_u32 m0, s41, 0x1000\n\t"
      "s_nop 0\n\t"
      "buffer_load_dwordx4 v62, s[28:31], s76 offen offset:2048 lds\n\t"
      "s_mov_b32 m0, s87\n\t"
      "s_nop 0\n\t"
      "ds_write_addtid_b32 v72 offset:28288\n\t"
      "s_waitcnt lgkmcnt(0)\n\t"
      "s_barrier\n\t"
      "ds_read_b128 v[64:67], v19 offset:17408\n\t"
      "ds_read_b128 v[68:71], v18 offset:0\n\t"
      "ds_read_b128 v[72:75], v20 offset:26112\n\t"
      "ds_read_b128 v[76:79], v19 offset:17472\n\t"
      "ds_read_b128 v[80:83], v18 offset:64\n\t"
      "ds_read_b128 v[84:87], v20 offset:26176\n\t"
      "ds_read_b128 v[88:91], v19 offset:17536\n\t"
      "ds_read_b128 v[92:95], v18 offset:128\n\t"
      "ds_read_b128 v[96:99], v20 offset:26240\n\t"
      "ds_read_b128 v[100:103], v19 offset:17600\n\t"
      "ds_read_b128 v[104:107], v18 offset:192\n\t"
      "ds_read_b128 v[108:111], v20 offset:26304\n\t"
      "s_waitcnt lgkmcnt(9)\n\t"
      "v_mfma_f32_16x16x32_bf16 v[160:163], v[64:67], v[68:71], 0\n\t"
      "v_mfma_f32_16x16x32_bf16 v[164:167], v[72:75], v[68:71], 0\n\t"
      "ds_read_b128 v[112:115], v19 offset:17664\n\t"
      "ds_read_b128 v[116:119], v18 offset:256\n\t"
      "ds_read_b128 v[120:123], v20 offset:26368\n\t"
      "s_waitcnt lgkmcnt(9)\n\t"
      "v_mfma_f32_16x16x32_bf16 v[160:163], v[76:79], v[80:83], v[160:163]\n\t"
      "v_mfma_f32_16x16x32_bf16 v[164:167], v[84:87], v[80:83], v[164:167]\n\t"
      "ds_read_b128 v[124:127], v19 offset:17728\n\t"
      "ds_read_b128 v[128:131], v18 offset:320\n\t"
      "ds_read_b128 v[132:135], v20 offset:26432\n\t"
      "s_waitcnt lgkmcnt(9)\n\t"
      "v_mfma_f32_16x16x32_bf16 v[160:163], v[88:91], v[92:95], v[160:163]\n\t"
      "v_mfma_f32_16x16x32_bf16 v[164:167], v[96:99], v[92:95], v[164:167]\n\t"
      "ds_read_b128 v[136:139], v19 offset:17792\n\t"
      "ds_read_b128 v[140:143], v18 offset:384\n\t"
      "ds_read_b128 v[144:147], v20 offset:26496\n\t"
      "s_waitcnt lgkmcnt(9)\n\t"
      "v_mfma_f32_16x16x32_bf16 v[160:163], v[100:103], v[104:107], v[160:163]\n\t"
      "v_mfma_f32_16x16x32_bf16 v[164:167], v[108:111], v[104:107], v[164:167]\n\t"
      "ds_read_b128 v[148:151], v19 offset:17856\n\t"
      "ds_read_b128 v[152:155], v18 offset:448\n\t"
      "ds_read_b128 v[156:159], v20 offset:26560\n\t"
      "s_waitcnt lgkmcnt(9)\n\t"
      "v_mfma_f32_16x16x32_bf16 v[160:163], v[112:115], v[116:119], v[160:163]\n\t"
      "v_mfma_f32_16x16x32_bf16 v[164:167], v[120:123], v[116:119], v[164:167]\n\t"
      "s_waitcnt lgkmcnt(6)\n\t"
      "v_mfma_f32_16x16x32_bf16 v[160:163], v[124:127], v[128:131], v[160:163]\n\t"
      "v_mfma_f32_16x16x32_bf16 v[164:167], v[132:135], v[128:131], v[164:167]\n\t"
      "s_waitcnt lgkmcnt(3)\n\t"
      "v_mfma_f32_16x16x32_bf16 v[160:163], v[136:139], v[140:143], v[160:163]\n\t"
      "v_mfma_f32_16x16x32_bf16 v[164:167], v[144:147], v[140:143], v[164:167]\n\t"
      "s_waitcnt lgkmcnt(0)\n\t"
      "v_mfma_f32_16x16x32_bf16 v[160:163], v[148:151], v[152:155], v[160:163]\n\t"
      "v_mfma_f32_16x16x32_bf16 v[164:167], v[156:159], v[152:155], v[164:167]\n\t"
      "s_mov_b64 exec, 1\n\t"
      "s_nop 5\n\t"
      "v_add_f32 v160, s42, v160\n\t"
      "s_mov_b64 exec, -1\n\t"
      "v_cvt_pk_bf16_f32 v160, v160, v161\n\t"
      "v_cvt_pk_bf16_f32 v161, v162, v163\n\t"
      "v_cvt_pk_bf16_f32 v164, v164, v165\n\t"
      "v_cvt_pk_bf16_f32 v165, v166, v167\n\t"
      "ds_write_b16 v13, v160\n\t"
      "ds_write_b16_d16_hi v14, v160\n\t"
      "ds_write_b16 v15, v161\n\t"
      "ds_write_b16_d16_hi v16, v161\n\t"
      "s_mov_b64 exec, s[66:67]\n\t"
      "ds_write_b16 v17, v164 offset:2112\n\t"
      "s_mov_b64 exec, 0xffff\n\t"
      "ds_write_b16_d16_hi v17, v164 offset:2640\n\t"
      "ds_write_b16 v17, v165 offset:3168\n\t"
      "ds_write_b16_d16_hi v17, v165 offset:3696\n\t"
      "s_mov_b64 exec, -1\n\t"
      "s_waitcnt lgkmcnt(0)\n\t"
      "s_barrier\n\t"
      "buffer_load_dwordx4 v[216:219], v21, s[8:11], s20 offen sc1\n\t"
      "buffer_load_dwordx4 v[220:223], v21, s[8:11], s21 offen sc1\n\t"
      "buffer_load_dwordx4 v[224:227], v21, s[8:11], s22 offen sc1\n\t"
      "buffer_load_dwordx4 v[228:231], v21, s[8:11], s23 offen sc1\n\t"
      "buffer_load_dwordx4 v[168:171], v21, s[8:11], s12 offen sc1\n\t"
      "buffer_load_dwordx4 v[172:175], v21, s[8:11], s13 offen sc1\n\t"
      "buffer_load_dwordx4 v[176:179], v253, s[8:11], s12 offen sc1\n\t"
      "buffer_load_dwordx4 v[180:183], v253, s[8:11], s13 offen sc1\n\t"
      "ds_read_u16 v64, v2 offset:60928\n\t"
      "ds_read_u16 v65, v3 offset:60928\n\t"
      "ds_read_u16 v66, v4 offset:60928\n\t"
      "ds_read_u16 v67, v5 offset:60928\n\t"
      "ds_read_u16 v68, v6 offset:60928\n\t"
      "ds_read_u16 v69, v7 offset:60928\n\t"
      "ds_read_u16 v70, v8 offset:60928\n\t"
      "ds_read_u16 v71, v9 offset:60928\n\t"
      "ds_read_u16 v72, v10 offset:60928\n\t"
      "ds_read_u16 v73, v11 offset:60928\n\t"
      "ds_read_u16 v74, v12 offset:60928\n\t"
      "v_mov_b32 v120, 0\n\t"
      "v_mov_b32 v121, 0\n\t"
      "v_mov_b32 v122, 0\n\t"
      "v_mov_b32 v123, 0\n\t"
      "ds_read_u16 v76, v2 offset:65168\n\t"
      "ds_read_u16 v77, v3 offset:65168\n\t"
      "ds_read_u16 v78, v4 offset:65168\n\t"
      "ds_read_u16 v79, v5 offset:65168\n\t"
      "ds_read_u16 v80, v6 offset:65168\n\t"
      "ds_read_u16 v81, v7 offset:65168\n\t"
      "ds_read_u16 v82, v8 offset:65168\n\t"
      "ds_read_u16 v83, v9 offset:65168\n\t"
      "ds_read_u16 v84, v10 offset:65168\n\t"
      "ds_read_u16 v85, v11 offset:65168\n\t"
      "ds_read_u16 v86, v12 offset:65168\n\t"
      "s_waitcnt lgkmcnt(11)\n\t"
      "v_lshl_or_b32 v144, v68, 16, v67\n\t"
      "v_lshl_or_b32 v145, v70, 16, v69\n\t"
      "v_lshl_or_b32 v146, v72, 16, v71\n\t"
      "v_lshl_or_b32 v147, v74, 16, v73\n\t"
      "ds_write_b128 v21, v[144:147] offset:2080\n\t"
      "v_lshl_or_b32 v140, v67, 16, v66\n\t"
      "v_lshl_or_b32 v141, v69, 16, v68\n\t"
      "v_lshl_or_b32 v142, v71, 16, v70\n\t"
      "v_lshl_or_b32 v143, v73, 16, v72\n\t"
      "ds_write_b128 v21, v[140:143] offset:6240\n\t"
      "v_lshl_or_b32 v134, v66, 16, v65\n\t"
      "v_lshl_or_b32 v135, v68, 16, v67\n\t"
      "v_lshl_or_b32 v136, v70, 16, v69\n\t"
      "v_lshl_or_b32 v137, v72, 16, v71\n\t"
      "ds_write_b128 v21, v[134:137] offset:10400\n\t"
      "v_lshl_or_b32 v128, v65, 16, v64\n\t"
      "v_lshl_or_b32 v129, v67, 16, v66\n\t"
      "v_lshl_or_b32 v130, v69, 16, v68\n\t"
      "v_lshl_or_b32 v131, v71, 16, v70\n\t"
      "ds_write_b128 v21, v[128:131] offset:14560\n\t"
      "s_waitcnt lgkmcnt(0)\n\t"
      "v_lshl_or_b32 v164, v80, 16, v79\n\t"
      "v_lshl_or_b32 v165, v82, 16, v81\n\t"
      "v_lshl_or_b32 v166, v84, 16, v83\n\t"
      "v_lshl_or_b32 v167, v86, 16, v85\n\t"
      "ds_write_b128 v21, v[164:167] offset:18720\n\t"
      "v_lshl_or_b32 v160, v79, 16, v78\n\t"
      "v_lshl_or_b32 v161, v81, 16, v80\n\t"
      "v_lshl_or_b32 v162, v83, 16, v82\n\t"
      "v_lshl_or_b32 v163, v85, 16, v84\n\t"
      "ds_write_b128 v21, v[160:163] offset:22880\n\t"
      "v_lshl_or_b32 v154, v78, 16, v77\n\t"
      "v_lshl_or_b32 v155, v80, 16, v79\n\t"
      "v_lshl_or_b32 v156, v82, 16, v81\n\t"
      "v_lshl_or_b32 v157, v84, 16, v83\n\t"
      "ds_write_b128 v21, v[154:157] offset:27040\n\t"
      "v_lshl_or_b32 v148, v77, 16, v76\n\t"
      "v_lshl_or_b32 v149, v79, 16, v78\n\t"
      "v_lshl_or_b32 v150, v81, 16, v80\n\t"
      "v_lshl_or_b32 v151, v83, 16, v82\n\t"
      "ds_write_b128 v21, v[148:151] offset:31200\n\t"
      "s_cmp_gt_u32 s45, 1\n\t"
      "s_cbranch_scc1 Lp1g%=\n\t"
      "s_movk_i32 s56, 4608\n\t"
      "s_mov_b32 s59, 35328\n\t"
      "s_cmp_eq_u32 s45, 0\n\t"
      "s_cselect_b32 s56, 4160, s56\n\t"
      "s_cselect_b32 s57, 1, 5\n\t"
      "s_cselect_b32 s58, 1, 31\n\t"
      "s_cselect_b32 s59, 2048, s59\n\t"
      "v_lshrrev_b32 v64, s57, v1\n\t"
      "v_and_b32 v65, s58, v1\n\t"
      "v_lshl_add_u32 v65, v65, 4, s59\n\t"
      "v_mad_u32_u24 v64, v64, s56, v65\n\t"
      "s_cselect_b64 exec, 0xffff, -1\n\t"
      "ds_write_b128 v64, v[120:123]\n\t"
      "s_mov_b64 exec, -1\n\t"
      "Lp1g%=:\n\t"
      "s_movk_i32 s73, 0x7fff\n\t"
      "s_waitcnt vmcnt(0)\n\t"
      "s_branch Ls1_chk%=\n\t"
      "Ls1_again%=:\n\t"
      "s_sleep 2\n\t"
      "buffer_load_dwordx4 v[216:219], v21, s[8:11], s20 offen sc1\n\t"
      "buffer_load_dwordx4 v[220:223], v21, s[8:11], s21 offen sc1\n\t"
      "buffer_load_dwordx4 v[224:227], v21, s[8:11], s22 offen sc1\n\t"
      "buffer_load_dwordx4 v[228:231], v21, s[8:11], s23 offen sc1\n\t"
      "buffer_load_dwordx4 v[168:171], v21, s[8:11], s12 offen sc1\n\t"
      "buffer_load_dwordx4 v[172:175], v21, s[8:11], s13 offen sc1\n\t"
      "buffer_load_dwordx4 v[176:179], v253, s[8:11], s12 offen sc1\n\t"
      "buffer_load_dwordx4 v[180:183], v253, s[8:11], s13 offen sc1\n\t"
      "s_waitcnt vmcnt(0)\n\t"
      "Ls1_chk%=:\n\t"
      "v_xor_b32 v64, s53, v217\n\t"
      "v_xor_b32 v65, s53, v219\n\t"
      "v_bitop3_b32 v66, v168, s18, v176 bitop3:0x96\n\t"
      "v_bitop3_b32 v67, v169, s18, v177 bitop3:0x96\n\t"
      "v_bitop3_b32 v68, v170, s18, v178 bitop3:0x96\n\t"
      "v_bitop3_b32 v69, v171, s18, v179 bitop3:0x96\n\t"
      "v_bitop3_b32 v70, v172, s18, v180 bitop3:0x96\n\t"
      "v_bitop3_b32 v71, v173, s18, v181 bitop3:0x96\n\t"
      "v_bitop3_b32 v72, v174, s18, v182 bitop3:0x96\n\t"
      "v_bitop3_b32 v73, v175, s18, v183 bitop3:0x96\n\t"
      "v_bitop3_b32 v64, v221, s53, v64 bitop3:0xbe\n\t"
      "v_bitop3_b32 v65, v223, s53, v65 bitop3:0xbe\n\t"
      "v_bitop3_b32 v64, v225, s53, v64 bitop3:0xbe\n\t"
      "v_bitop3_b32 v65, v227, s53, v65 bitop3:0xbe\n\t"
      "v_bitop3_b32 v64, v229, s53, v64 bitop3:0xbe\n\t"
      "v_bitop3_b32 v65, v231, s53, v65 bitop3:0xbe\n\t"
      "v_or3_b32 v66, v66, v67, v68\n\t"
      "v_or3_b32 v69, v69, v70, v71\n\t"
      "v_or3_b32 v64, v64, v72, v73\n\t"
      "v_or3_b32 v65, v65, v66, v69\n\t"
      "v_or_b32 v64, v64, v65\n\t"
      "v_cmp_ne_u32 vcc, 0, v64\n\t"
      "v_and_b32 v72, 0xffff0000, v216\n\t"
      "v_and_b32 v73, 0xffff0000, v218\n\t"
      "v_and_b32 v74, 0xffff0000, v220\n\t"
      "v_and_b32 v75, 0xffff0000, v222\n\t"
      "v_and_b32 v76, 0xffff0000, v224\n\t"
      "v_and_b32 v77, 0xffff0000, v226\n\t"
      "v_and_b32 v78, 0xffff0000, v228\n\t"
      "v_and_b32 v79, 0xffff0000, v230\n\t"
      "s_sub_u32 s73, s73, 1\n\t"
      "s_cselect_b64 s[88:89], 0, vcc\n\t"
      "s_cmp_lg_u64 s[88:89], 0\n\t"
      "s_cbranch_scc1 Ls1_again%=\n\t"
      "v_rcp_f32 v72, v72\n\t"
      "v_lshlrev_b32 v80, 16, v216\n\t"
      "v_rcp_f32 v73, v73\n\t"
      "v_lshlrev_b32 v81, 16, v218\n\t"
      "v_rcp_f32 v74, v74\n\t"
      "v_lshlrev_b32 v82, 16, v220\n\t"
      "v_rcp_f32 v75, v75\n\t"
      "v_lshlrev_b32 v83, 16, v222\n\t"
      "v_rcp_f32 v76, v76\n\t"
      "v_lshlrev_b32 v84, 16, v224\n\t"
      "v_rcp_f32 v77, v77\n\t"
      "v_lshlrev_b32 v85, 16, v226\n\t"
      "v_rcp_f32 v78, v78\n\t"
      "v_lshlrev_b32 v86, 16, v228\n\t"
      "v_rcp_f32 v79, v79\n\t"
      "v_lshlrev_b32 v87, 16, v230\n\t"
      "v_lshlrev_b32 v96, 16, v168\n\t"
      "v_and_b32 v97, 0xffff0000, v168\n\t"
      "v_lshlrev_b32 v98, 16, v169\n\t"
      "v_and_b32 v99, 0xffff0000, v169\n\t"
      "v_lshlrev_b32 v100, 16, v170\n\t"
      "v_and_b32 v101, 0xffff0000, v170\n\t"
      "v_lshlrev_b32 v102, 16, v171\n\t"
      "v_and_b32 v103, 0xffff0000, v171\n\t"
      "v_mul_f32 v80, -v80, v72\n\t"
      "v_mul_f32 v81, -v81, v73\n\t"
      "v_mul_f32 v82, -v82, v74\n\t"
      "v_mul_f32 v83, -v83, v75\n\t"
      "v_mul_f32 v84, -v84, v76\n\t"
      "v_mul_f32 v85, -v85, v77\n\t"
      "v_mul_f32 v86, -v86, v78\n\t"
      "v_mul_f32 v87, -v87, v79\n\t"
      "v_pk_fma_f32 v[96:97], v[96:97], v[72:73], v[80:81]\n\t"
      "v_pk_fma_f32 v[98:99], v[98:99], v[74:75], v[82:83]\n\t"
      "v_pk_fma_f32 v[100:101], v[100:101], v[76:77], v[84:85]\n\t"
      "v_pk_fma_f32 v[102:103], v[102:103], v[78:79], v[86:87]\n\t"
      "v_cvt_pk_bf16_f32 v88, v96, v97\n\t"
      "v_cvt_pk_bf16_f32 v89, v98, v99\n\t"
      "v_cvt_pk_bf16_f32 v90, v100, v101\n\t"
      "v_cvt_pk_bf16_f32 v91, v102, v103\n\t"
      "ds_write_b128 v21, v[88:91] offset:35840\n\t"
      "v_lshlrev_b32 v96, 16, v172\n\t"
      "v_and_b32 v97, 0xffff0000, v172\n\t"
      "v_lshlrev_b32 v98, 16, v173\n\t"
      "v_and_b32 v99, 0xffff0000, v173\n\t"
      "v_lshlrev_b32 v100, 16, v174\n\t"
      "v_and_b32 v101, 0xffff0000, v174\n\t"
      "v_lshlrev_b32 v102, 16, v175\n\t"
      "v_and_b32 v103, 0xffff0000, v175\n\t"
      "v_pk_fma_f32 v[96:97], v[96:97], v[72:73], v[80:81]\n\t"
      "v_pk_fma_f32 v[98:99], v[98:99], v[74:75], v[82:83]\n\t"
      "v_pk_fma_f32 v[100:101], v[100:101], v[76:77], v[84:85]\n\t"
      "v_pk_fma_f32 v[102:103], v[102:103], v[78:79], v[86:87]\n\t"
      "v_cvt_pk_bf16_f32 v92, v96, v97\n\t"
      "v_cvt_pk_bf16_f32 v93, v98, v99\n\t"
      "v_cvt_pk_bf16_f32 v94, v100, v101\n\t"
      "v_cvt_pk_bf16_f32 v95, v102, v103\n\t"
      "ds_write_b128 v21, v[92:95] offset:40448\n\t"
      "s_memrealtime s[26:27]\n\t"
      "s_waitcnt lgkmcnt(0)\n\t"
      "s_barrier\n\t"
      "s_lshl_b32 s56, s44, 3\n\t"
      "v_mov_b32 v243, s26\n\t"
      "s_add_u32 s56, s56, 24576\n\t"
      "s_cmp_eq_u32 s45, 0\n\t"
      "s_cselect_b64 exec, 1, 0\n\t"
      "buffer_store_dwordx2 v[242:243], off, s[8:11], s56 sc1\n\t"
      "s_mov_b64 exec, -1\n\t"
      "ds_read_b64_tr_b16 v[160:161], v29 offset:8\n\t"
      "ds_read_b64_tr_b16 v[162:163], v29 offset:0\n\t"
      "ds_read_b128 v[96:99], v28 offset:192\n\t"
      "ds_read_b128 v[100:103], v28 offset:704\n\t"
      "ds_read_b128 v[104:107], v28 offset:1216\n\t"
      "ds_read_b128 v[108:111], v28 offset:1728\n\t"
      "ds_read_b128 v[112:115], v28 offset:2240\n\t"
      "ds_read_b128 v[116:119], v28 offset:2752\n\t"
      "ds_read_b128 v[120:123], v28 offset:3264\n\t"
      "ds_read_b128 v[124:127], v28 offset:3776\n\t"
      "ds_read_b64_tr_b16 v[164:165], v29 offset:520\n\t"
      "ds_read_b64_tr_b16 v[166:167], v29 offset:512\n\t"
      "s_waitcnt lgkmcnt(9)\n\t"
      "v_mfma_f32_16x16x32_bf16 v[64:67], v[160:163], v[96:99], 0\n\t"
      "s_waitcnt lgkmcnt(8)\n\t"
      "v_mfma_f32_16x16x32_bf16 v[68:71], v[160:163], v[100:103], 0\n\t"
      "s_waitcnt lgkmcnt(7)\n\t"
      "v_mfma_f32_16x16x32_bf16 v[72:75], v[160:163], v[104:107], 0\n\t"
      "s_waitcnt lgkmcnt(6)\n\t"
      "v_mfma_f32_16x16x32_bf16 v[76:79], v[160:163], v[108:111], 0\n\t"
      "s_waitcnt lgkmcnt(5)\n\t"
      "v_mfma_f32_16x16x32_bf16 v[80:83], v[160:163], v[112:115], 0\n\t"
      "ds_read_b128 v[128:131], v28 offset:128\n\t"
      "s_waitcnt lgkmcnt(5)\n\t"
      "v_mfma_f32_16x16x32_bf16 v[84:87], v[160:163], v[116:119], 0\n\t"
      "ds_read_b128 v[132:135], v28 offset:640\n\t"
      "s_waitcnt lgkmcnt(5)\n\t"
      "v_mfma_f32_16x16x32_bf16 v[88:91], v[160:163], v[120:123], 0\n\t"
      "ds_read_b64_tr_b16 v[168:169], v29 offset:1032\n\t"
      "s_waitcnt lgkmcnt(3)\n\t"
      "v_mfma_f32_16x16x32_bf16 v[92:95], v[160:163], v[124:127], 0\n\t"
      "ds_read_b64_tr_b16 v[170:171], v29 offset:1024\n\t"
      "v_mfma_f32_16x16x32_bf16 v[68:71], v[164:167], v[96:99], v[68:71]\n\t"
      "buffer_load_dwordx2 v[204:205], v27, s[8:11], s14 offen sc1\n\t"
      "v_mfma_f32_16x16x32_bf16 v[72:75], v[164:167], v[100:103], v[72:75]\n\t"
      "buffer_load_dwordx2 v[206:207], v27, s[8:11], s14 offen offset:512 sc1\n\t"
      "v_mfma_f32_16x16x32_bf16 v[76:79], v[164:167], v[104:107], v[76:79]\n\t"
      "ds_read_b128 v[136:139], v28 offset:1152\n\t"
      "v_mfma_f32_16x16x32_bf16 v[80:83], v[164:167], v[108:111], v[80:83]\n\t"
      "ds_read_b128 v[140:143], v28 offset:1664\n\t"
      "v_mfma_f32_16x16x32_bf16 v[84:87], v[164:167], v[112:115], v[84:87]\n\t"
      "ds_read_b64_tr_b16 v[172:173], v29 offset:1544\n\t"
      "v_mfma_f32_16x16x32_bf16 v[88:91], v[164:167], v[116:119], v[88:91]\n\t"
      "ds_read_b64_tr_b16 v[174:175], v29 offset:1536\n\t"
      "v_mfma_f32_16x16x32_bf16 v[92:95], v[164:167], v[120:123], v[92:95]\n\t"
      "buffer_load_dwordx2 v[208:209], v27, s[8:11], s14 offen offset:1024 sc1\n\t"
      "s_waitcnt lgkmcnt(4)\n\t"
      "v_mfma_f32_16x16x32_bf16 v[72:75], v[168:171], v[96:99], v[72:75]\n\t"
      "ds_read_b128 v[144:147], v28 offset:2176\n\t"
      "v_mfma_f32_16x16x32_bf16 v[76:79], v[168:171], v[100:103], v[76:79]\n\t"
      "ds_read_b128 v[148:151], v28 offset:2688\n\t"
      "v_mfma_f32_16x16x32_bf16 v[80:83], v[168:171], v[104:107], v[80:83]\n\t"
      "ds_read_b64_tr_b16 v[176:177], v29 offset:2056\n\t"
      "v_mfma_f32_16x16x32_bf16 v[84:87], v[168:171], v[108:111], v[84:87]\n\t"
      "ds_read_b64_tr_b16 v[178:179], v29 offset:2048\n\t"
      "v_mfma_f32_16x16x32_bf16 v[88:91], v[168:171], v[112:115], v[88:91]\n\t"
      "ds_read_b128 v[152:155], v28 offset:3200\n\t"
      "v_mfma_f32_16x16x32_bf16 v[92:95], v[168:171], v[116:119], v[92:95]\n\t"
      "ds_read_b128 v[156:159], v28 offset:3712\n\t"
      "s_waitcnt lgkmcnt(6)\n\t"
      "v_mfma_f32_16x16x32_bf16 v[76:79], v[172:175], v[96:99], v[76:79]\n\t"
      "ds_read_b64_tr_b16 v[160:161], v29 offset:2568\n\t"
      "v_mfma_f32_16x16x32_bf16 v[80:83], v[172:175], v[100:103], v[80:83]\n\t"
      "ds_read_b64_tr_b16 v[162:163], v29 offset:2560\n\t"
      "v_mfma_f32_16x16x32_bf16 v[84:87], v[172:175], v[104:107], v[84:87]\n\t"
      "ds_read_b64_tr_b16 v[164:165], v29 offset:3080\n\t"
      "v_mfma_f32_16x16x32_bf16 v[88:91], v[172:175], v[108:111], v[88:91]\n\t"
      "ds_read_b64_tr_b16 v[166:167], v29 offset:3072\n\t"
      "v_mfma_f32_16x16x32_bf16 v[92:95], v[172:175], v[112:115], v[92:95]\n\t"
      "ds_read_b64_tr_b16 v[168:169], v29 offset:3592\n\t"
      "s_waitcnt lgkmcnt(7)\n\t"
      "v_mfma_f32_16x16x32_bf16 v[80:83], v[176:179], v[96:99], v[80:83]\n\t"
      "ds_read_b64_tr_b16 v[170:171], v29 offset:3584\n\t"
      "v_mfma_f32_16x16x32_bf16 v[84:87], v[176:179], v[100:103], v[84:87]\n\t"
      "ds_read_b64_tr_b16 v[172:173], v29 offset:72\n\t"
      "v_mfma_f32_16x16x32_bf16 v[88:91], v[176:179], v[104:107], v[88:91]\n\t"
      "ds_read_b64_tr_b16 v[174:175], v29 offset:64\n\t"
      "v_mfma_f32_16x16x32_bf16 v[92:95], v[176:179], v[108:111], v[92:95]\n\t"
      "buffer_load_dwordx2 v[210:211], v27, s[8:11], s14 offen offset:1536 sc1\n\t"
      "s_add_u32 s41, s41, 0x7040\n\t"
      "s_waitcnt lgkmcnt(6)\n\t"
      "v_mfma_f32_16x16x32_bf16 v[84:87], v[160:163], v[96:99], v[84:87]\n\t"
      "s_add_u32 s76, s76, 0x70000\n\t"
      "s_add_u32 m0, s41, 0x0\n\t"
      "v_mfma_f32_16x16x32_bf16 v[88:91], v[160:163], v[100:103], v[88:91]\n\t"
      "v_mfma_f32_16x16x32_bf16 v[92:95], v[160:163], v[104:107], v[92:95]\n\t"
      "s_waitcnt lgkmcnt(3)\n\t"
      "v_mfma_f32_16x16x32_bf16 v[88:91], v[164:167], v[96:99], v[88:91]\n\t"
      "ds_read_b64_tr_b16 v[176:177], v29 offset:584\n\t"
      "v_mfma_f32_16x16x32_bf16 v[92:95], v[164:167], v[100:103], v[92:95]\n\t"
      "ds_read_b64_tr_b16 v[178:179], v29 offset:576\n\t"
      "s_waitcnt lgkmcnt(3)\n\t"
      "v_mfma_f32_16x16x32_bf16 v[92:95], v[168:171], v[96:99], v[92:95]\n\t"
      "buffer_load_dwordx4 v56, s[28:31], s76 offen lds\n\t"
      "s_waitcnt lgkmcnt(2)\n\t"
      "v_mfma_f32_16x16x32_bf16 v[64:67], v[172:175], v[128:131], v[64:67]\n\t"
      "v_mfma_f32_16x16x32_bf16 v[68:71], v[172:175], v[132:135], v[68:71]\n\t"
      "v_mfma_f32_16x16x32_bf16 v[72:75], v[172:175], v[136:139], v[72:75]\n\t"
      "v_mfma_f32_16x16x32_bf16 v[76:79], v[172:175], v[140:143], v[76:79]\n\t"
      "buffer_load_dwordx4 v57, s[28:31], s76 offen offset:1024 lds\n\t"
      "v_mfma_f32_16x16x32_bf16 v[80:83], v[172:175], v[144:147], v[80:83]\n\t"
      "ds_read_b128 v[96:99], v28 offset:64\n\t"
      "v_mfma_f32_16x16x32_bf16 v[84:87], v[172:175], v[148:151], v[84:87]\n\t"
      "ds_read_b128 v[100:103], v28 offset:576\n\t"
      "v_mfma_f32_16x16x32_bf16 v[88:91], v[172:175], v[152:155], v[88:91]\n\t"
      "ds_read_b64_tr_b16 v[160:161], v29 offset:1096\n\t"
      "v_mfma_f32_16x16x32_bf16 v[92:95], v[172:175], v[156:159], v[92:95]\n\t"
      "ds_read_b64_tr_b16 v[162:163], v29 offset:1088\n\t"
      "s_waitcnt lgkmcnt(4)\n\t"
      "v_mfma_f32_16x16x32_bf16 v[68:71], v[176:179], v[128:131], v[68:71]\n\t"
      "buffer_load_dwordx4 v58, s[28:31], s76 offen offset:2048 lds\n\t"
      "v_mfma_f32_16x16x32_bf16 v[72:75], v[176:179], v[132:135], v[72:75]\n\t"
      "v_mfma_f32_16x16x32_bf16 v[76:79], v[176:179], v[136:139], v[76:79]\n\t"
      "ds_read_b128 v[104:107], v28 offset:1088\n\t"
      "v_mfma_f32_16x16x32_bf16 v[80:83], v[176:179], v[140:143], v[80:83]\n\t"
      "ds_read_b128 v[108:111], v28 offset:1600\n\t"
      "v_mfma_f32_16x16x32_bf16 v[84:87], v[176:179], v[144:147], v[84:87]\n\t"
      "ds_read_b64_tr_b16 v[164:165], v29 offset:1608\n\t"
      "v_mfma_f32_16x16x32_bf16 v[88:91], v[176:179], v[148:151], v[88:91]\n\t"
      "ds_read_b64_tr_b16 v[166:167], v29 offset:1600\n\t"
      "v_mfma_f32_16x16x32_bf16 v[92:95], v[176:179], v[152:155], v[92:95]\n\t"
      "buffer_load_dwordx4 v59, s[28:31], s76 offen offset:3072 lds\n\t"
      "s_add_u32 m0, s41, 0x1000\n\t"
      "s_waitcnt lgkmcnt(4)\n\t"
      "v_mfma_f32_16x16x32_bf16 v[72:75], v[160:163], v[128:131], v[72:75]\n\t"
      "ds_read_b128 v[112:115], v28 offset:2112\n\t"
      "v_mfma_f32_16x16x32_bf16 v[76:79], v[160:163], v[132:135], v[76:79]\n\t"
      "ds_read_b128 v[116:119], v28 offset:2624\n\t"
      "v_mfma_f32_16x16x32_bf16 v[80:83], v[160:163], v[136:139], v[80:83]\n\t"
      "ds_read_b64_tr_b16 v[168:169], v29 offset:2120\n\t"
      "v_mfma_f32_16x16x32_bf16 v[84:87], v[160:163], v[140:143], v[84:87]\n\t"
      "ds_read_b64_tr_b16 v[170:171], v29 offset:2112\n\t"
      "v_mfma_f32_16x16x32_bf16 v[88:91], v[160:163], v[144:147], v[88:91]\n\t"
      "ds_read_b128 v[120:123], v28 offset:3136\n\t"
      "v_mfma_f32_16x16x32_bf16 v[92:95], v[160:163], v[148:151], v[92:95]\n\t"
      "ds_read_b128 v[124:127], v28 offset:3648\n\t"
      "s_waitcnt lgkmcnt(6)\n\t"
      "v_mfma_f32_16x16x32_bf16 v[76:79], v[164:167], v[128:131], v[76:79]\n\t"
      "ds_read_b64_tr_b16 v[172:173], v29 offset:2632\n\t"
      "v_mfma_f32_16x16x32_bf16 v[80:83], v[164:167], v[132:135], v[80:83]\n\t"
      "ds_read_b64_tr_b16 v[174:175], v29 offset:2624\n\t"
      "v_mfma_f32_16x16x32_bf16 v[84:87], v[164:167], v[136:139], v[84:87]\n\t"
      "ds_read_b64_tr_b16 v[176:177], v29 offset:3144\n\t"
      "v_mfma_f32_16x16x32_bf16 v[88:91], v[164:167], v[140:143], v[88:91]\n\t"
      "ds_read_b64_tr_b16 v[178:179], v29 offset:3136\n\t"
      "v_mfma_f32_16x16x32_bf16 v[92:95], v[164:167], v[144:147], v[92:95]\n\t"
      "ds_read_b64_tr_b16 v[160:161], v29 offset:3656\n\t"
      "s_waitcnt lgkmcnt(7)\n\t"
      "v_mfma_f32_16x16x32_bf16 v[80:83], v[168:171], v[128:131], v[80:83]\n\t"
      "ds_read_b64_tr_b16 v[162:163], v29 offset:3648\n\t"
      "v_mfma_f32_16x16x32_bf16 v[84:87], v[168:171], v[132:135], v[84:87]\n\t"
      "ds_read_b64_tr_b16 v[164:165], v29 offset:136\n\t"
      "v_mfma_f32_16x16x32_bf16 v[88:91], v[168:171], v[136:139], v[88:91]\n\t"
      "ds_read_b64_tr_b16 v[166:167], v29 offset:128\n\t"
      "v_mfma_f32_16x16x32_bf16 v[92:95], v[168:171], v[140:143], v[92:95]\n\t"
      "buffer_load_dwordx4 v60, s[28:31], s76 offen lds\n\t"
      "s_waitcnt lgkmcnt(6)\n\t"
      "v_mfma_f32_16x16x32_bf16 v[84:87], v[172:175], v[128:131], v[84:87]\n\t"
      "v_mfma_f32_16x16x32_bf16 v[88:91], v[172:175], v[132:135], v[88:91]\n\t"
      "v_mfma_f32_16x16x32_bf16 v[92:95], v[172:175], v[136:139], v[92:95]\n\t"
      "s_waitcnt lgkmcnt(3)\n\t"
      "v_mfma_f32_16x16x32_bf16 v[88:91], v[176:179], v[128:131], v[88:91]\n\t"
      "ds_read_b64_tr_b16 v[168:169], v29 offset:648\n\t"
      "v_mfma_f32_16x16x32_bf16 v[92:95], v[176:179], v[132:135], v[92:95]\n\t"
      "ds_read_b64_tr_b16 v[170:171], v29 offset:640\n\t"
      "s_waitcnt lgkmcnt(3)\n\t"
      "v_mfma_f32_16x16x32_bf16 v[92:95], v[160:163], v[128:131], v[92:95]\n\t"
      "buffer_load_dwordx4 v61, s[28:31], s76 offen offset:1024 lds\n\t"
      "s_waitcnt lgkmcnt(2)\n\t"
      "v_mfma_f32_16x16x32_bf16 v[64:67], v[164:167], v[96:99], v[64:67]\n\t"
      "v_mfma_f32_16x16x32_bf16 v[68:71], v[164:167], v[100:103], v[68:71]\n\t"
      "v_mfma_f32_16x16x32_bf16 v[72:75], v[164:167], v[104:107], v[72:75]\n\t"
      "v_mfma_f32_16x16x32_bf16 v[76:79], v[164:167], v[108:111], v[76:79]\n\t"
      "buffer_load_dwordx4 v62, s[28:31], s76 offen offset:2048 lds\n\t"
      "s_add_u32 s41, s41, 0x7040\n\t"
      "v_mfma_f32_16x16x32_bf16 v[80:83], v[164:167], v[112:115], v[80:83]\n\t"
      "ds_read_b128 v[128:131], v28 offset:0\n\t"
      "v_mfma_f32_16x16x32_bf16 v[84:87], v[164:167], v[116:119], v[84:87]\n\t"
      "ds_read_b128 v[132:135], v28 offset:512\n\t"
      "v_mfma_f32_16x16x32_bf16 v[88:91], v[164:167], v[120:123], v[88:91]\n\t"
      "ds_read_b64_tr_b16 v[172:173], v29 offset:1160\n\t"
      "v_mfma_f32_16x16x32_bf16 v[92:95], v[164:167], v[124:127], v[92:95]\n\t"
      "ds_read_b64_tr_b16 v[174:175], v29 offset:1152\n\t"
      "s_waitcnt lgkmcnt(4)\n\t"
      "v_mfma_f32_16x16x32_bf16 v[68:71], v[168:171], v[96:99], v[68:71]\n\t"
      "s_add_u32 s76, s76, 0x70000\n\t"
      "s_add_u32 m0, s41, 0x0\n\t"
      "v_mfma_f32_16x16x32_bf16 v[72:75], v[168:171], v[100:103], v[72:75]\n\t"
      "buffer_load_dwordx4 v56, s[28:31], s76 offen lds\n\t"
      "v_mfma_f32_16x16x32_bf16 v[76:79], v[168:171], v[104:107], v[76:79]\n\t"
      "ds_read_b128 v[136:139], v28 offset:1024\n\t"
      "v_mfma_f32_16x16x32_bf16 v[80:83], v[168:171], v[108:111], v[80:83]\n\t"
      "ds_read_b128 v[140:143], v28 offset:1536\n\t"
      "v_mfma_f32_16x16x32_bf16 v[84:87], v[168:171], v[112:115], v[84:87]\n\t"
      "ds_read_b64_tr_b16 v[176:177], v29 offset:1672\n\t"
      "v_mfma_f32_16x16x32_bf16 v[88:91], v[168:171], v[116:119], v[88:91]\n\t"
      "ds_read_b64_tr_b16 v[178:179], v29 offset:1664\n\t"
      "v_mfma_f32_16x16x32_bf16 v[92:95], v[168:171], v[120:123], v[92:95]\n\t"
      "buffer_load_dwordx4 v57, s[28:31], s76 offen offset:1024 lds\n\t"
      "s_waitcnt lgkmcnt(4)\n\t"
      "v_mfma_f32_16x16x32_bf16 v[72:75], v[172:175], v[96:99], v[72:75]\n\t"
      "ds_read_b128 v[144:147], v28 offset:2048\n\t"
      "v_mfma_f32_16x16x32_bf16 v[76:79], v[172:175], v[100:103], v[76:79]\n\t"
      "ds_read_b128 v[148:151], v28 offset:2560\n\t"
      "v_mfma_f32_16x16x32_bf16 v[80:83], v[172:175], v[104:107], v[80:83]\n\t"
      "ds_read_b64_tr_b16 v[160:161], v29 offset:2184\n\t"
      "v_mfma_f32_16x16x32_bf16 v[84:87], v[172:175], v[108:111], v[84:87]\n\t"
      "ds_read_b64_tr_b16 v[162:163], v29 offset:2176\n\t"
      "v_mfma_f32_16x16x32_bf16 v[88:91], v[172:175], v[112:115], v[88:91]\n\t"
      "ds_read_b128 v[152:155], v28 offset:3072\n\t"
      "v_mfma_f32_16x16x32_bf16 v[92:95], v[172:175], v[116:119], v[92:95]\n\t"
      "ds_read_b128 v[156:159], v28 offset:3584\n\t"
      "s_waitcnt lgkmcnt(6)\n\t"
      "v_mfma_f32_16x16x32_bf16 v[76:79], v[176:179], v[96:99], v[76:79]\n\t"
      "ds_read_b64_tr_b16 v[164:165], v29 offset:2696\n\t"
      "v_mfma_f32_16x16x32_bf16 v[80:83], v[176:179], v[100:103], v[80:83]\n\t"
      "ds_read_b64_tr_b16 v[166:167], v29 offset:2688\n\t"
      "v_mfma_f32_16x16x32_bf16 v[84:87], v[176:179], v[104:107], v[84:87]\n\t"
      "ds_read_b64_tr_b16 v[168:169], v29 offset:3208\n\t"
      "v_mfma_f32_16x16x32_bf16 v[88:91], v[176:179], v[108:111], v[88:91]\n\t"
      "ds_read_b64_tr_b16 v[170:171], v29 offset:3200\n\t"
      "v_mfma_f32_16x16x32_bf16 v[92:95], v[176:179], v[112:115], v[92:95]\n\t"
      "ds_read_b64_tr_b16 v[172:173], v29 offset:3720\n\t"
      "s_waitcnt lgkmcnt(7)\n\t"
      "v_mfma_f32_16x16x32_bf16 v[80:83], v[160:163], v[96:99], v[80:83]\n\t"
      "ds_read_b64_tr_b16 v[174:175], v29 offset:3712\n\t"
      "v_mfma_f32_16x16x32_bf16 v[84:87], v[160:163], v[100:103], v[84:87]\n\t"
      "ds_read_b64_tr_b16 v[176:177], v29 offset:200\n\t"
      "v_mfma_f32_16x16x32_bf16 v[88:91], v[160:163], v[104:107], v[88:91]\n\t"
      "ds_read_b64_tr_b16 v[178:179], v29 offset:192\n\t"
      "v_mfma_f32_16x16x32_bf16 v[92:95], v[160:163], v[108:111], v[92:95]\n\t"
      "buffer_load_dwordx4 v58, s[28:31], s76 offen offset:2048 lds\n\t"
      "s_waitcnt lgkmcnt(6)\n\t"
      "v_mfma_f32_16x16x32_bf16 v[84:87], v[164:167], v[96:99], v[84:87]\n\t"
      "v_mfma_f32_16x16x32_bf16 v[88:91], v[164:167], v[100:103], v[88:91]\n\t"
      "v_mfma_f32_16x16x32_bf16 v[92:95], v[164:167], v[104:107], v[92:95]\n\t"
      "s_waitcnt lgkmcnt(3)\n\t"
      "v_mfma_f32_16x16x32_bf16 v[88:91], v[168:171], v[96:99], v[88:91]\n\t"
      "ds_read_b64_tr_b16 v[160:161], v29 offset:712\n\t"
      "v_mfma_f32_16x16x32_bf16 v[92:95], v[168:171], v[100:103], v[92:95]\n\t"
      "ds_read_b64_tr_b16 v[162:163], v29 offset:704\n\t"
      "s_waitcnt lgkmcnt(3)\n\t"
      "v_mfma_f32_16x16x32_bf16 v[92:95], v[172:175], v[96:99], v[92:95]\n\t"
      "buffer_load_dwordx4 v59, s[28:31], s76 offen offset:3072 lds\n\t"
      "s_add_u32 m0, s41, 0x1000\n\t"
      "s_waitcnt lgkmcnt(2)\n\t"
      "v_mfma_f32_16x16x32_bf16 v[64:67], v[176:179], v[128:131], v[64:67]\n\t"
      "v_mfma_f32_16x16x32_bf16 v[68:71], v[176:179], v[132:135], v[68:71]\n\t"
      "v_mfma_f32_16x16x32_bf16 v[72:75], v[176:179], v[136:139], v[72:75]\n\t"
      "v_mfma_f32_16x16x32_bf16 v[76:79], v[176:179], v[140:143], v[76:79]\n\t"
      "buffer_load_dwordx4 v60, s[28:31], s76 offen lds\n\t"
      "v_mfma_f32_16x16x32_bf16 v[80:83], v[176:179], v[144:147], v[80:83]\n\t"
      "ds_read_b64_tr_b16 v[164:165], v29 offset:1224\n\t"
      "v_mfma_f32_16x16x32_bf16 v[84:87], v[176:179], v[148:151], v[84:87]\n\t"
      "ds_read_b64_tr_b16 v[166:167], v29 offset:1216\n\t"
      "v_mfma_f32_16x16x32_bf16 v[88:91], v[176:179], v[152:155], v[88:91]\n\t"
      "v_mfma_f32_16x16x32_bf16 v[92:95], v[176:179], v[156:159], v[92:95]\n\t"
      "buffer_load_dwordx4 v61, s[28:31], s76 offen offset:1024 lds\n\t"
      "s_waitcnt lgkmcnt(2)\n\t"
      "v_mfma_f32_16x16x32_bf16 v[68:71], v[160:163], v[128:131], v[68:71]\n\t"
      "v_mfma_f32_16x16x32_bf16 v[72:75], v[160:163], v[132:135], v[72:75]\n\t"
      "v_mfma_f32_16x16x32_bf16 v[76:79], v[160:163], v[136:139], v[76:79]\n\t"
      "ds_read_b64_tr_b16 v[168:169], v29 offset:1736\n\t"
      "v_mfma_f32_16x16x32_bf16 v[80:83], v[160:163], v[140:143], v[80:83]\n\t"
      "ds_read_b64_tr_b16 v[170:171], v29 offset:1728\n\t"
      "v_mfma_f32_16x16x32_bf16 v[84:87], v[160:163], v[144:147], v[84:87]\n\t"
      "buffer_load_dwordx4 v62, s[28:31], s76 offen offset:2048 lds\n\t"
      "s_add_u32 m0, s15, 0x0\n\t"
      "v_mfma_f32_16x16x32_bf16 v[88:91], v[160:163], v[148:151], v[88:91]\n\t"
      "v_mfma_f32_16x16x32_bf16 v[92:95], v[160:163], v[152:155], v[92:95]\n\t"
      "s_waitcnt lgkmcnt(2)\n\t"
      "v_mfma_f32_16x16x32_bf16 v[72:75], v[164:167], v[128:131], v[72:75]\n\t"
      "ds_read_b64_tr_b16 v[172:173], v29 offset:2248\n\t"
      "v_mfma_f32_16x16x32_bf16 v[76:79], v[164:167], v[132:135], v[76:79]\n\t"
      "ds_read_b64_tr_b16 v[174:175], v29 offset:2240\n\t"
      "v_mfma_f32_16x16x32_bf16 v[80:83], v[164:167], v[136:139], v[80:83]\n\t"
      "buffer_load_dwordx4 v56, s[28:31], s16 offen lds\n\t"
      "v_mfma_f32_16x16x32_bf16 v[84:87], v[164:167], v[140:143], v[84:87]\n\t"
      "v_mfma_f32_16x16x32_bf16 v[88:91], v[164:167], v[144:147], v[88:91]\n\t"
      "ds_read_b64_tr_b16 v[176:177], v29 offset:2760\n\t"
      "v_mfma_f32_16x16x32_bf16 v[92:95], v[164:167], v[148:151], v[92:95]\n\t"
      "ds_read_b64_tr_b16 v[178:179], v29 offset:2752\n\t"
      "s_waitcnt lgkmcnt(4)\n\t"
      "v_mfma_f32_16x16x32_bf16 v[76:79], v[168:171], v[128:131], v[76:79]\n\t"
      "buffer_load_dwordx4 v57, s[28:31], s16 offen offset:1024 lds\n\t"
      "v_mfma_f32_16x16x32_bf16 v[80:83], v[168:171], v[132:135], v[80:83]\n\t"
      "ds_read_b64_tr_b16 v[160:161], v29 offset:3272\n\t"
      "v_mfma_f32_16x16x32_bf16 v[84:87], v[168:171], v[136:139], v[84:87]\n\t"
      "ds_read_b64_tr_b16 v[162:163], v29 offset:3264\n\t"
      "v_mfma_f32_16x16x32_bf16 v[88:91], v[168:171], v[140:143], v[88:91]\n\t"
      "ds_read_b64_tr_b16 v[164:165], v29 offset:3784\n\t"
      "v_mfma_f32_16x16x32_bf16 v[92:95], v[168:171], v[144:147], v[92:95]\n\t"
      "ds_read_b64_tr_b16 v[166:167], v29 offset:3776\n\t"
      "s_waitcnt lgkmcnt(6)\n\t"
      "v_mfma_f32_16x16x32_bf16 v[80:83], v[172:175], v[128:131], v[80:83]\n\t"
      "buffer_load_dwordx4 v58, s[28:31], s16 offen offset:2048 lds\n\t"
      "v_mfma_f32_16x16x32_bf16 v[84:87], v[172:175], v[132:135], v[84:87]\n\t"
      "v_mfma_f32_16x16x32_bf16 v[88:91], v[172:175], v[136:139], v[88:91]\n\t"
      "v_mfma_f32_16x16x32_bf16 v[92:95], v[172:175], v[140:143], v[92:95]\n\t"
      "s_waitcnt lgkmcnt(3)\n\t"
      "v_mfma_f32_16x16x32_bf16 v[84:87], v[176:179], v[128:131], v[84:87]\n\t"
      "buffer_load_dwordx4 v59, s[28:31], s16 offen offset:3072 lds\n\t"
      "s_add_u32 m0, s15, 0x1000\n\t"
      "v_mfma_f32_16x16x32_bf16 v[88:91], v[176:179], v[132:135], v[88:91]\n\t"
      "v_mfma_f32_16x16x32_bf16 v[92:95], v[176:179], v[136:139], v[92:95]\n\t"
      "s_waitcnt lgkmcnt(0)\n\t"
      "v_mfma_f32_16x16x32_bf16 v[88:91], v[160:163], v[128:131], v[88:91]\n\t"
      "v_mfma_f32_16x16x32_bf16 v[92:95], v[160:163], v[132:135], v[92:95]\n\t"
      "buffer_load_dwordx4 v60, s[28:31], s16 offen lds\n\t"
      "v_mfma_f32_16x16x32_bf16 v[92:95], v[164:167], v[128:131], v[92:95]\n\t"
      "buffer_load_dwordx4 v61, s[28:31], s16 offen offset:1024 lds\n\t"
      "buffer_load_dwordx4 v62, s[28:31], s16 offen offset:2048 lds\n\t"
      "buffer_load_dwordx2 v[244:245], v255, s[8:11], s24 offen sc1\n\t"
      "s_waitcnt vmcnt(22)\n\t"
      "s_cmp_eq_u32 s47, 0\n\t"
      "s_barrier\n\t"
      "s_cbranch_scc1 Lp2a%=\n\t"
      "ds_write_b128 v31, v[64:67] offset:2048\n\t"
      "ds_write_b128 v31, v[68:71] offset:3072\n\t"
      "ds_write_b128 v31, v[72:75] offset:4096\n\t"
      "ds_write_b128 v31, v[76:79] offset:5120\n\t"
      "s_branch Lp2b%=\n\t"
      "Lp2a%=:\n\t"
      "ds_write_b128 v31, v[80:83] offset:2048\n\t"
      "ds_write_b128 v31, v[84:87] offset:3072\n\t"
      "ds_write_b128 v31, v[88:91] offset:4096\n\t"
      "ds_write_b128 v31, v[92:95] offset:5120\n\t"
      "Lp2b%=:\n\t"
      "v_lshlrev_b32 v112, 16, v204\n\t"
      "v_and_b32 v113, 0xffff0000, v204\n\t"
      "v_lshlrev_b32 v114, 16, v205\n\t"
      "v_and_b32 v115, 0xffff0000, v205\n\t"
      "v_lshlrev_b32 v116, 16, v206\n\t"
      "v_and_b32 v117, 0xffff0000, v206\n\t"
      "v_lshlrev_b32 v118, 16, v207\n\t"
      "v_and_b32 v119, 0xffff0000, v207\n\t"
      "v_lshlrev_b32 v120, 16, v208\n\t"
      "v_and_b32 v121, 0xffff0000, v208\n\t"
      "v_lshlrev_b32 v122, 16, v209\n\t"
      "v_and_b32 v123, 0xffff0000, v209\n\t"
      "v_lshlrev_b32 v124, 16, v210\n\t"
      "v_and_b32 v125, 0xffff0000, v210\n\t"
      "v_lshlrev_b32 v126, 16, v211\n\t"
      "v_and_b32 v127, 0xffff0000, v211\n\t"
      "s_waitcnt lgkmcnt(0)\n\t"
      "s_barrier\n\t"
      "ds_read_b128 v[96:99], v32 offset:2048\n\t"
      "ds_read_b128 v[100:103], v32 offset:3072\n\t"
      "ds_read_b128 v[104:107], v32 offset:4096\n\t"
      "ds_read_b128 v[108:111], v32 offset:5120\n\t"
      "s_cbranch_scc1 Lp2c%=\n\t"
      "v_pk_add_f32 v[112:113], v[112:113], v[80:81]\n\t"
      "v_pk_add_f32 v[114:115], v[114:115], v[82:83]\n\t"
      "v_pk_add_f32 v[116:117], v[116:117], v[84:85]\n\t"
      "v_pk_add_f32 v[118:119], v[118:119], v[86:87]\n\t"
      "v_pk_add_f32 v[120:121], v[120:121], v[88:89]\n\t"
      "v_pk_add_f32 v[122:123], v[122:123], v[90:91]\n\t"
      "v_pk_add_f32 v[124:125], v[124:125], v[92:93]\n\t"
      "v_pk_add_f32 v[126:127], v[126:127], v[94:95]\n\t"
      "s_branch Lp2d%=\n\t"
      "Lp2c%=:\n\t"
      "v_pk_add_f32 v[112:113], v[112:113], v[64:65]\n\t"
      "v_pk_add_f32 v[114:115], v[114:115], v[66:67]\n\t"
      "v_pk_add_f32 v[116:117], v[116:117], v[68:69]\n\t"
      "v_pk_add_f32 v[118:119], v[118:119], v[70:71]\n\t"
      "v_pk_add_f32 v[120:121], v[120:121], v[72:73]\n\t"
      "v_pk_add_f32 v[122:123], v[122:123], v[74:75]\n\t"
      "v_pk_add_f32 v[124:125], v[124:125], v[76:77]\n\t"
      "v_pk_add_f32 v[126:127], v[126:127], v[78:79]\n\t"
      "Lp2d%=:\n\t"
      "s_waitcnt lgkmcnt(3)\n\t"
      "v_pk_add_f32 v[112:113], v[112:113], v[96:97]\n\t"
      "v_pk_add_f32 v[114:115], v[114:115], v[98:99]\n\t"
      "v_cvt_pk_bf16_f32 v64, v112, v113\n\t"
      "v_cvt_pk_bf16_f32 v65, v114, v115\n\t"
      "buffer_store_dwordx2 v[64:65], v27, s[8:11], s81 offen sc1\n\t"
      "s_waitcnt lgkmcnt(2)\n\t"
      "v_pk_add_f32 v[116:117], v[116:117], v[100:101]\n\t"
      "v_pk_add_f32 v[118:119], v[118:119], v[102:103]\n\t"
      "v_cvt_pk_bf16_f32 v66, v116, v117\n\t"
      "v_cvt_pk_bf16_f32 v67, v118, v119\n\t"
      "buffer_store_dwordx2 v[66:67], v27, s[8:11], s81 offen offset:512 sc1\n\t"
      "s_waitcnt lgkmcnt(1)\n\t"
      "v_pk_add_f32 v[120:121], v[120:121], v[104:105]\n\t"
      "v_pk_add_f32 v[122:123], v[122:123], v[106:107]\n\t"
      "v_cvt_pk_bf16_f32 v68, v120, v121\n\t"
      "v_cvt_pk_bf16_f32 v69, v122, v123\n\t"
      "buffer_store_dwordx2 v[68:69], v27, s[8:11], s81 offen offset:1024 sc1\n\t"
      "s_waitcnt lgkmcnt(0)\n\t"
      "v_pk_add_f32 v[124:125], v[124:125], v[108:109]\n\t"
      "v_pk_add_f32 v[126:127], v[126:127], v[110:111]\n\t"
      "v_cvt_pk_bf16_f32 v70, v124, v125\n\t"
      "v_cvt_pk_bf16_f32 v71, v126, v127\n\t"
      "buffer_store_dwordx2 v[70:71], v27, s[8:11], s81 offen offset:1536 sc1\n\t"
      "s_memrealtime s[24:25]\n\t"
      "s_movk_i32 s59, 7184\n\t"
      "v_mul_u32_u24 v96, s59, v1\n\t"
      "s_mov_b64 exec, 7\n\t"
      "ds_write_b128 v96, v[52:55] offset:51712\n\t"
      "s_mov_b64 exec, -1\n\t"
      "s_movk_i32 s68, 24576\n\t"
      "s_waitcnt vmcnt(4)\n\t"
      "s_movk_i32 s73, 0x7fff\n\t"
      "s_branch Lt2_chk%=\n\t"
      "Lt2_again%=:\n\t"
      "s_sleep 8\n\t"
      "buffer_load_dwordx2 v[244:245], v255, s[8:11], s68 offen sc1\n\t"
      "s_waitcnt vmcnt(0)\n\t"
      "Lt2_chk%=:\n\t"
      "v_cmp_ne_u32 vcc, s53, v244\n\t"
      "v_subrev_u32 v245, s26, v245\n\t"
      "s_sub_u32 s73, s73, 1\n\t"
      "s_cselect_b64 s[88:89], 0, vcc\n\t"
      "s_cmp_lg_u64 s[88:89], 0\n\t"
      "s_cbranch_scc1 Lt2_again%=\n\t"
      "v_max_i32_dpp v245, v245, v245 row_shr:1 row_mask:0xf bank_mask:0xf\n\t"
      "s_nop 1\n\t"
      "v_max_i32_dpp v245, v245, v245 row_shr:2 row_mask:0xf bank_mask:0xf\n\t"
      "s_nop 1\n\t"
      "v_max_i32_dpp v245, v245, v245 row_shr:4 row_mask:0xf bank_mask:0xf\n\t"
      "s_nop 1\n\t"
      "v_max_i32_dpp v245, v245, v245 row_shr:8 row_mask:0xf bank_mask:0xf\n\t"
      "s_mov_b32 exec_lo, 0x80008000\n\t"
      "s_mov_b32 exec_hi, 0x80008000\n\t"
      "ds_max_i32 v246, v245\n\t"
      "s_mov_b64 exec, -1\n\t"
      "s_waitcnt lgkmcnt(0)\n\t"
      "s_barrier\n\t"
      "s_add_u32 m0, s52, 0x800\n\t"
      "s_add_u32 s55, s54, 0x0\n\t"
      "buffer_load_dwordx4 v34, s[32:35], s55 offen lds\n\t"
      "v_add_u32 v23, 0xffff9e00, v23\n\t"
      "v_add_u32 v24, 0xffff9e00, v24\n\t"
      "v_add_u32 v25, 0xffff9e00, v25\n\t"
      "v_add_u32 v26, 0xffff9e00, v26\n\t"
      "ds_read_b64_tr_b16 a[96:97], v23 offset:0\n\t"
      "ds_read_b64_tr_b16 a[98:99], v24 offset:0\n\t"
      "ds_read_b64_tr_b16 a[100:101], v25 offset:0\n\t"
      "ds_read_b64_tr_b16 a[102:103], v26 offset:0\n\t"
      "s_add_u32 s55, s54, 0xdfc00\n\t"
      "buffer_load_dwordx4 v34, s[32:35], s55 offen offset:1024 lds\n\t"
      "ds_read_b64_tr_b16 a[104:105], v23 offset:7184\n\t"
      "ds_read_b64_tr_b16 a[106:107], v24 offset:7184\n\t"
      "ds_read_b64_tr_b16 a[108:109], v25 offset:7184\n\t"
      "ds_read_b64_tr_b16 a[110:111], v26 offset:7184\n\t"
      "ds_read_b64_tr_b16 a[112:113], v23 offset:14368\n\t"
      "ds_read_b64_tr_b16 a[114:115], v24 offset:14368\n\t"
      "ds_read_b64_tr_b16 a[116:117], v25 offset:14368\n\t"
      "ds_read_b64_tr_b16 a[118:119], v26 offset:14368\n\t"
      "s_add_u32 s55, s54, 0x7800\n\t"
      "buffer_load_dwordx4 v34, s[32:35], s55 offen offset:2048 lds\n\t"
      "ds_read_b64_tr_b16 a[0:1], v23 offset:25088\n\t"
      "ds_read_b64_tr_b16 a[2:3], v24 offset:25088\n\t"
      "ds_read_b64_tr_b16 a[4:5], v25 offset:25088\n\t"
      "ds_read_b64_tr_b16 a[6:7], v26 offset:25088\n\t"
      "ds_read_b64_tr_b16 a[8:9], v23 offset:32272\n\t"
      "ds_read_b64_tr_b16 a[10:11], v24 offset:32272\n\t"
      "ds_read_b64_tr_b16 a[12:13], v25 offset:32272\n\t"
      "ds_read_b64_tr_b16 a[14:15], v26 offset:32272\n\t"
      "s_add_u32 s55, s54, 0xe7400\n\t"
      "buffer_load_dwordx4 v34, s[32:35], s55 offen offset:3072 lds\n\t"
      "ds_read_b64_tr_b16 a[16:17], v23 offset:39456\n\t"
      "ds_read_b64_tr_b16 a[18:19], v24 offset:39456\n\t"
      "ds_read_b64_tr_b16 a[20:21], v25 offset:39456\n\t"
      "ds_read_b64_tr_b16 a[22:23], v26 offset:39456\n\t"
      "ds_read_b64_tr_b16 a[24:25], v23 offset:46640\n\t"
      "ds_read_b64_tr_b16 a[26:27], v24 offset:46640\n\t"
      "ds_read_b64_tr_b16 a[28:29], v25 offset:46640\n\t"
      "ds_read_b64_tr_b16 a[30:31], v26 offset:46640\n\t"
      "s_add_u32 m0, s52, 0x4800\n\t"
      "s_add_u32 s55, s54, 0x10000\n\t"
      "buffer_load_dwordx4 v34, s[32:35], s55 offen lds\n\t"
      "ds_read_b64_tr_b16 a[32:33], v23 offset:53824\n\t"
      "ds_read_b64_tr_b16 a[34:35], v24 offset:53824\n\t"
      "ds_read_b64_tr_b16 a[36:37], v25 offset:53824\n\t"
      "ds_read_b64_tr_b16 a[38:39], v26 offset:53824\n\t"
      "ds_read_b64_tr_b16 a[40:41], v23 offset:61008\n\t"
      "ds_read_b64_tr_b16 a[42:43], v24 offset:61008\n\t"
      "ds_read_b64_tr_b16 a[44:45], v25 offset:61008\n\t"
      "ds_read_b64_tr_b16 a[46:47], v26 offset:61008\n\t"
      "s_add_u32 s55, s54, 0xefc00\n\t"
      "buffer_load_dwordx4 v34, s[32:35], s55 offen offset:1024 lds\n\t"
      "v_add_u32 v23, 0x10a60, v23\n\t"
      "v_add_u32 v24, 0x10a60, v24\n\t"
      "v_add_u32 v25, 0x10a60, v25\n\t"
      "v_add_u32 v26, 0x10a60, v26\n\t"
      "ds_read_b64_tr_b16 a[48:49], v23 offset:0\n\t"
      "ds_read_b64_tr_b16 a[50:51], v24 offset:0\n\t"
      "ds_read_b64_tr_b16 a[52:53], v25 offset:0\n\t"
      "ds_read_b64_tr_b16 a[54:55], v26 offset:0\n\t"
      "s_add_u32 s55, s54, 0x17800\n\t"
      "buffer_load_dwordx4 v34, s[32:35], s55 offen offset:2048 lds\n\t"
      "ds_read_b64_tr_b16 a[56:57], v23 offset:7184\n\t"
      "ds_read_b64_tr_b16 a[58:59], v24 offset:7184\n\t"
      "ds_read_b64_tr_b16 a[60:61], v25 offset:7184\n\t"
      "ds_read_b64_tr_b16 a[62:63], v26 offset:7184\n\t"
      "ds_read_b64_tr_b16 a[64:65], v23 offset:14368\n\t"
      "ds_read_b64_tr_b16 a[66:67], v24 offset:14368\n\t"
      "ds_read_b64_tr_b16 a[68:69], v25 offset:14368\n\t"
      "ds_read_b64_tr_b16 a[70:71], v26 offset:14368\n\t"
      "s_add_u32 s55, s54, 0xf7400\n\t"
      "buffer_load_dwordx4 v34, s[32:35], s55 offen offset:3072 lds\n\t"
      "ds_read_b64_tr_b16 a[72:73], v23 offset:21552\n\t"
      "ds_read_b64_tr_b16 a[74:75], v24 offset:21552\n\t"
      "ds_read_b64_tr_b16 a[76:77], v25 offset:21552\n\t"
      "ds_read_b64_tr_b16 a[78:79], v26 offset:21552\n\t"
      "ds_read_b64_tr_b16 a[80:81], v23 offset:28736\n\t"
      "ds_read_b64_tr_b16 a[82:83], v24 offset:28736\n\t"
      "ds_read_b64_tr_b16 a[84:85], v25 offset:28736\n\t"
      "ds_read_b64_tr_b16 a[86:87], v26 offset:28736\n\t"
      "ds_read_b64_tr_b16 a[88:89], v23 offset:35920\n\t"
      "ds_read_b64_tr_b16 a[90:91], v24 offset:35920\n\t"
      "ds_read_b64_tr_b16 a[92:93], v25 offset:35920\n\t"
      "ds_read_b64_tr_b16 a[94:95], v26 offset:35920\n\t"
      "ds_read_b64_tr_b16 a[120:121], v23 offset:43104\n\t"
      "ds_read_b64_tr_b16 a[122:123], v24 offset:43104\n\t"
      "ds_read_b64_tr_b16 a[124:125], v25 offset:43104\n\t"
      "ds_read_b64_tr_b16 a[126:127], v26 offset:43104\n\t"
      "ds_read_b32 v245, v246\n\t"
      "s_waitcnt lgkmcnt(0)\n\t"
      "v_readfirstlane_b32 s56, v245\n\t"
      "s_nop 0\n\t"
      "s_min_u32 s56, s56, 400\n\t"
      "s_add_u32 s56, s56, 45\n\t"
      "Lu2%=:\n\t"
      "s_memrealtime s[88:89]\n\t"
      "s_waitcnt lgkmcnt(0)\n\t"
      "s_sub_u32 s88, s88, s24\n\t"
      "s_cmp_lt_u32 s88, s56\n\t"
      "s_cbranch_scc1 Lu2%=\n\t"
      "s_memrealtime s[26:27]\n\t"
      "v_mov_b32 v72, 0\n\t"
      "s_waitcnt vmcnt(8)\n\t"
      "s_waitcnt lgkmcnt(0)\n\t"
      "s_barrier\n\t"
      "ds_write_b32 v246, v72\n\t"
      "s_cmp_lg_u32 s45, 0\n\t"
      "s_cbranch_scc1 Lf2d%=\n\t"
      "s_lshl_b32 s56, s44, 4\n\t"
      "s_add_u32 s56, s56, 196608\n\t"
      "s_mov_b64 exec, 1\n\t"
      "buffer_store_dword v242, off, s[8:11], s56 sc1\n\t"
      "s_cmp_lg_u32 s44, 0\n\t"
      "s_cbranch_scc1 Lf2dx%=\n\t"
      "buffer_store_dword v242, off, s[8:11], 0 sc1\n\t"
      "Lf2dx%=:\n\t"
      "s_mov_b64 exec, -1\n\t"
      "Lf2d%=:\n\t"
      "s_lshl_b32 s56, s44, 3\n\t"
      "v_mov_b32 v243, s26\n\t"
      "s_add_u32 s56, s56, 28672\n\t"
      "s_cmp_eq_u32 s45, 0\n\t"
      "s_cselect_b64 exec, 1, 0\n\t"
      "buffer_store_dwordx2 v[242:243], off, s[8:11], s56 sc1\n\t"
      "s_mov_b64 exec, -1\n\t"
      "Lg1%=:\n\t"
      "s_add_u32 m0, s52, 0x8800\n\t"
      "s_add_u32 s55, s82, 0x0\n\t"
      "buffer_load_dwordx4 v196, s[8:11], s55 offen sc1 lds\n\t"
      "v_mfma_f32_32x32x16_bf16 v[160:175], a[0:3], v[52:55], 0\n\t"
      "buffer_load_dwordx4 v197, s[8:11], s55 offen offset:1024 sc1 lds\n\t"
      "v_mfma_f32_32x32x16_bf16 v[160:175], a[4:7], v[52:55], v[160:175]\n\t"
      "buffer_load_dwordx4 v198, s[8:11], s55 offen offset:2048 sc1 lds\n\t"
      "v_mfma_f32_32x32x16_bf16 v[160:175], a[8:11], v[52:55], v[160:175]\n\t"
      "buffer_load_dwordx4 v199, s[8:11], s55 offen offset:3072 sc1 lds\n\t"
      "v_mfma_f32_32x32x16_bf16 v[160:175], a[12:15], v[52:55], v[160:175]\n\t"
      "s_add_u32 m0, s52, 0xc800\n\t"
      "s_add_u32 s55, s82, 0x44000\n\t"
      "buffer_load_dwordx4 v196, s[8:11], s55 offen sc1 lds\n\t"
      "v_mfma_f32_32x32x16_bf16 v[160:175], a[16:19], v[52:55], v[160:175]\n\t"
      "buffer_load_dwordx4 v197, s[8:11], s55 offen offset:1024 sc1 lds\n\t"
      "v_mfma_f32_32x32x16_bf16 v[160:175], a[20:23], v[52:55], v[160:175]\n\t"
      "buffer_load_dwordx4 v198, s[8:11], s55 offen offset:2048 sc1 lds\n\t"
      "v_mfma_f32_32x32x16_bf16 v[160:175], a[24:27], v[52:55], v[160:175]\n\t"
      "buffer_load_dwordx4 v199, s[8:11], s55 offen offset:3072 sc1 lds\n\t"
      "v_mfma_f32_32x32x16_bf16 v[160:175], a[28:31], v[52:55], v[160:175]\n\t"
      "s_add_u32 m0, s52, 0x10800\n\t"
      "s_add_u32 s55, s82, 0x88000\n\t"
      "buffer_load_dwordx4 v196, s[8:11], s55 offen sc1 lds\n\t"
      "v_mfma_f32_32x32x16_bf16 v[160:175], a[32:35], v[52:55], v[160:175]\n\t"
      "s_mul_i32 s57, s51, 278528\n\t"
      "s_add_u32 s84, s82, s57\n\t"
      "s_lshl_b32 s57, s50, 7\n\t"
      "v_mfma_f32_32x32x16_bf16 v[160:175], a[36:39], v[52:55], v[160:175]\n\t"
      "s_add_u32 s84, s84, s57\n\t"
      "s_lshl_b32 s83, s45, 11\n\t"
      "s_add_u32 s83, s83, 0x24800\n\t"
      "buffer_load_dwordx4 v197, s[8:11], s55 offen offset:1024 sc1 lds\n\t"
      "v_mfma_f32_32x32x16_bf16 v[160:175], a[40:43], v[52:55], v[160:175]\n\t"
      "v_and_b32 v72, 31, v0\n\t"
      "v_lshlrev_b32 v72, 2, v72\n\t"
      "s_lshl_b32 s56, s72, 7\n\t"
      "v_mfma_f32_32x32x16_bf16 v[160:175], a[44:47], v[52:55], v[160:175]\n\t"
      "v_add_u32 v38, s56, v72\n\t"
      "v_xor_b32 v39, 0x80, v38\n\t"
      "v_xor_b32 v40, 0x100, v38\n\t"
      "buffer_load_dwordx4 v198, s[8:11], s55 offen offset:2048 sc1 lds\n\t"
      "v_mfma_f32_32x32x16_bf16 v[160:175], a[48:51], v[52:55], v[160:175]\n\t"
      "v_xor_b32 v41, 0x180, v38\n\t"
      "v_lshlrev_b32 v42, 2, v0\n\t"
      "s_lshl_b32 s56, s46, 9\n\t"
      "v_mfma_f32_32x32x16_bf16 v[160:175], a[52:55], v[52:55], v[160:175]\n\t"
      "v_add_u32 v43, s56, v72\n\t"
      "v_lshlrev_b32 v44, 4, v213\n\t"
      "s_lshl_b32 s86, s48, 8\n\t"
      "buffer_load_dwordx4 v199, s[8:11], s55 offen offset:3072 sc1 lds\n\t"
      "v_mfma_f32_32x32x16_bf16 v[160:175], a[56:59], v[52:55], v[160:175]\n\t"
      "s_add_u32 s86, s86, 131072\n\t"
      "s_lshl_b32 s56, s46, 13\n\t"
      "s_add_u32 s56, s56, 2048\n\t"
      "v_mfma_f32_32x32x16_bf16 v[160:175], a[60:63], v[52:55], v[160:175]\n\t"
      "v_add_u32 v200, s56, v30\n\t"
      "v_add_u32 v201, 0x10000, v200\n\t"
      "v_add_u32 v202, 0x20000, v200\n\t"
      "v_mfma_f32_32x32x16_bf16 v[160:175], a[64:67], v[52:55], v[160:175]\n\t"
      "s_lshl_b32 s56, s48, 2\n\t"
      "s_lshl_b32 s57, s50, 1\n\t"
      "s_add_u32 s56, s56, s57\n\t"
      "v_mfma_f32_32x32x16_bf16 v[160:175], a[68:71], v[52:55], v[160:175]\n\t"
      "s_add_u32 s57, s56, s47\n\t"
      "s_mul_i32 s57, s57, 0x1c000\n\t"
      "s_mul_i32 s58, s46, 0xe000\n\t"
      "v_mfma_f32_32x32x16_bf16 v[160:175], a[72:75], v[52:55], v[160:175]\n\t"
      "s_add_u32 s85, s57, s58\n\t"
      "s_add_u32 s85, s85, 0x800000\n\t"
      "s_add_u32 s56, s56, s46\n\t"
      "v_mfma_f32_32x32x16_bf16 v[160:175], a[76:79], v[52:55], v[160:175]\n\t"
      "s_lshl_b32 s56, s56, 15\n\t"
      "s_lshl_b32 s57, s51, 7\n\t"
      "s_add_u32 s56, s56, s57\n\t"
      "v_mfma_f32_32x32x16_bf16 v[160:175], a[80:83], v[52:55], v[160:175]\n\t"
      "s_lshl_b32 s57, s47, 6\n\t"
      "s_add_u32 s56, s56, s57\n\t"
      "v_bfe_u32 v73, v0, 5, 1\n\t"
      "v_mfma_f32_32x32x16_bf16 v[160:175], a[84:87], v[52:55], v[160:175]\n\t"
      "v_lshlrev_b32 v73, 4, v73\n\t"
      "v_lshl_or_b32 v73, v72, 8, v73\n\t"
      "v_add_u32 v45, s56, v73\n\t"
      "v_mfma_f32_32x32x16_bf16 v[160:175], a[88:91], v[52:55], v[160:175]\n\t"
      "v_mfma_f32_32x32x16_bf16 v[160:175], a[92:95], v[52:55], v[160:175]\n\t"
      "v_mfma_f32_32x32x16_bf16 v[160:175], a[96:99], v[52:55], v[160:175]\n\t"
      "v_mfma_f32_32x32x16_bf16 v[160:175], a[100:103], v[52:55], v[160:175]\n\t"
      "v_mfma_f32_32x32x16_bf16 v[160:175], a[104:107], v[52:55], v[160:175]\n\t"
      "v_mfma_f32_32x32x16_bf16 v[160:175], a[108:111], v[52:55], v[160:175]\n\t"
      "v_mfma_f32_32x32x16_bf16 v[160:175], a[112:115], v[52:55], v[160:175]\n\t"
      "v_mfma_f32_32x32x16_bf16 v[160:175], a[116:119], v[52:55], v[160:175]\n\t"
      "v_mfma_f32_32x32x16_bf16 v[160:175], a[120:123], v[52:55], v[160:175]\n\t"
      "v_mfma_f32_32x32x16_bf16 v[160:175], a[124:127], v[52:55], v[160:175]\n\t"
      "s_waitcnt vmcnt(8)\n\t"
      "s_barrier\n\t"
      "ds_read_b64_tr_b16 v[136:137], v188 offset:32768\n\t"
      "ds_read_b64_tr_b16 v[138:139], v188 offset:33024\n\t"
      "ds_read_b64_tr_b16 v[140:141], v189 offset:32768\n\t"
      "ds_read_b64_tr_b16 v[142:143], v189 offset:33024\n\t"
      "ds_read_b64_tr_b16 v[144:145], v190 offset:32768\n\t"
      "ds_read_b64_tr_b16 v[146:147], v190 offset:33024\n\t"
      "ds_read_b64_tr_b16 v[148:149], v191 offset:32768\n\t"
      "ds_read_b64_tr_b16 v[150:151], v191 offset:33024\n\t"
      "ds_read_b64_tr_b16 v[152:153], v192 offset:32768\n\t"
      "ds_read_b64_tr_b16 v[154:155], v192 offset:33024\n\t"
      "s_waitcnt lgkmcnt(8)\n\t"
      "v_mfma_f32_32x32x16_bf16 v[64:79], a[0:3], v[136:139], 0\n\t"
      "ds_read_b64_tr_b16 v[156:157], v193 offset:32768\n\t"
      "ds_read_b64_tr_b16 v[158:159], v193 offset:33024\n\t"
      "v_add_u32 v63, 0x800, v35\n\t"
      "ds_read_b64_tr_b16 a[128:129], v63 offset:0\n\t"
      "s_add_u32 m0, s52, 0x14800\n\t"
      "s_add_u32 s55, s82, 0xcc000\n\t"
      "buffer_load_dwordx4 v196, s[8:11], s55 offen sc1 lds\n\t"
      "v_mfma_f32_4x4x4_16b_bf16 v[128:131], v[136:137], v[136:137], 0\n\t"
      "v_mfma_f32_4x4x4_16b_bf16 v[132:135], v[138:139], v[138:139], 0\n\t"
      "s_waitcnt lgkmcnt(9)\n\t"
      "v_mfma_f32_32x32x16_bf16 v[80:95], a[0:3], v[140:143], 0\n\t"
      "ds_read_b64_tr_b16 v[136:137], v194 offset:32768\n\t"
      "ds_read_b64_tr_b16 v[138:139], v194 offset:33024\n\t"
      "ds_read_b64_tr_b16 a[130:131], v63 offset:4096\n\t"
      "s_waitcnt lgkmcnt(10)\n\t"
      "v_mfma_f32_32x32x16_bf16 v[96:111], a[0:3], v[144:147], 0\n\t"
      "ds_read_b64_tr_b16 v[140:141], v195 offset:32768\n\t"
      "ds_read_b64_tr_b16 v[142:143], v195 offset:33024\n\t"
      "ds_read_b64_tr_b16 a[132:133], v63 offset:8192\n\t"
      "buffer_load_dwordx4 v197, s[8:11], s55 offen offset:1024 sc1 lds\n\t"
      "s_waitcnt lgkmcnt(11)\n\t"
      "v_mfma_f32_32x32x16_bf16 v[112:127], a[0:3], v[148:151], 0\n\t"
      "ds_read_b64_tr_b16 v[144:145], v188 offset:40960\n\t"
      "ds_read_b64_tr_b16 v[146:147], v188 offset:41216\n\t"
      "ds_read_b64_tr_b16 a[134:135], v63 offset:12288\n\t"
      "s_waitcnt lgkmcnt(12)\n\t"
      "v_mfma_f32_32x32x16_bf16 v[64:79], a[4:7], v[152:155], v[64:79]\n\t"
      "ds_read_b64_tr_b16 v[148:149], v189 offset:40960\n\t"
      "ds_read_b64_tr_b16 v[150:151], v189 offset:41216\n\t"
      "ds_read_b64_tr_b16 a[136:137], v63 offset:2048\n\t"
      "buffer_load_dwordx4 v198, s[8:11], s55 offen offset:2048 sc1 lds\n\t"
      "s_waitcnt lgkmcnt(13)\n\t"
      "v_mfma_f32_32x32x16_bf16 v[80:95], a[4:7], v[156:159], v[80:95]\n\t"
      "ds_read_b64_tr_b16 v[152:153], v190 offset:40960\n\t"
      "ds_read_b64_tr_b16 v[154:155], v190 offset:41216\n\t"
      "ds_read_b64_tr_b16 a[138:139], v63 offset:6144\n\t"
      "s_waitcnt lgkmcnt(13)\n\t"
      "v_mfma_f32_32x32x16_bf16 v[96:111], a[4:7], v[136:139], v[96:111]\n\t"
      "ds_read_b64_tr_b16 v[156:157], v191 offset:40960\n\t"
      "ds_read_b64_tr_b16 v[158:159], v191 offset:41216\n\t"
      "ds_read_b64_tr_b16 a[140:141], v63 offset:10240\n\t"
      "buffer_load_dwordx4 v199, s[8:11], s55 offen offset:3072 sc1 lds\n\t"
      "s_add_u32 m0, s52, 0x20800\n\t"
      "s_add_u32 s55, s54, 0x20000\n\t"
      "s_waitcnt lgkmcnt(13)\n\t"
      "v_mfma_f32_32x32x16_bf16 v[112:127], a[4:7], v[140:143], v[112:127]\n\t"
      "ds_read_b64_tr_b16 v[136:137], v192 offset:40960\n\t"
      "ds_read_b64_tr_b16 v[138:139], v192 offset:41216\n\t"
      "ds_read_b64_tr_b16 a[142:143], v63 offset:14336\n\t"
      "s_waitcnt lgkmcnt(13)\n\t"
      "v_mfma_f32_32x32x16_bf16 v[64:79], a[8:11], v[144:147], v[64:79]\n\t"
      "ds_read_b64_tr_b16 v[140:141], v193 offset:40960\n\t"
      "ds_read_b64_tr_b16 v[142:143], v193 offset:41216\n\t"
      "ds_read_b64_tr_b16 a[144:145], v63 offset:16384\n\t"
      "buffer_load_dwordx4 v34, s[32:35], s55 offen lds\n\t"
      "s_add_u32 s55, s54, 0xffc00\n\t"
      "v_mfma_f32_4x4x4_16b_bf16 v[128:131], v[144:145], v[144:145], v[128:131]\n\t"
      "v_mfma_f32_4x4x4_16b_bf16 v[132:135], v[146:147], v[146:147], v[132:135]\n\t"
      "s_waitcnt lgkmcnt(13)\n\t"
      "v_mfma_f32_32x32x16_bf16 v[80:95], a[8:11], v[148:151], v[80:95]\n\t"
      "ds_read_b64_tr_b16 v[144:145], v194 offset:40960\n\t"
      "ds_read_b64_tr_b16 v[146:147], v194 offset:41216\n\t"
      "ds_read_b64_tr_b16 a[146:147], v63 offset:20480\n\t"
      "s_waitcnt lgkmcnt(13)\n\t"
      "v_mfma_f32_32x32x16_bf16 v[96:111], a[8:11], v[152:155], v[96:111]\n\t"
      "s_waitcnt vmcnt(9)\n\t"
      "s_barrier\n\t"
      "ds_read_b64_tr_b16 v[148:149], v195 offset:40960\n\t"
      "ds_read_b64_tr_b16 v[150:151], v195 offset:41216\n\t"
      "ds_read_b64_tr_b16 a[148:149], v63 offset:24576\n\t"
      "buffer_load_dwordx4 v34, s[32:35], s55 offen offset:1024 lds\n\t"
      "s_add_u32 s55, s54, 0x27800\n\t"
      "s_waitcnt lgkmcnt(13)\n\t"
      "v_mfma_f32_32x32x16_bf16 v[112:127], a[8:11], v[156:159], v[112:127]\n\t"
      "ds_read_b64_tr_b16 v[152:153], v188 offset:49152\n\t"
      "ds_read_b64_tr_b16 v[154:155], v188 offset:49408\n\t"
      "ds_read_b64_tr_b16 a[150:151], v63 offset:28672\n\t"
      "s_waitcnt lgkmcnt(13)\n\t"
      "v_mfma_f32_32x32x16_bf16 v[64:79], a[12:15], v[136:139], v[64:79]\n\t"
      "ds_read_b64_tr_b16 v[156:157], v189 offset:49152\n\t"
      "ds_read_b64_tr_b16 v[158:159], v189 offset:49408\n\t"
      "ds_read_b64_tr_b16 a[152:153], v63 offset:18432\n\t"
      "buffer_load_dwordx4 v34, s[32:35], s55 offen offset:2048 lds\n\t"
      "s_add_u32 s55, s54, 0x107400\n\t"
      "s_waitcnt lgkmcnt(13)\n\t"
      "v_mfma_f32_32x32x16_bf16 v[80:95], a[12:15], v[140:143], v[80:95]\n\t"
      "ds_read_b64_tr_b16 v[136:137], v190 offset:49152\n\t"
      "ds_read_b64_tr_b16 v[138:139], v190 offset:49408\n\t"
      "ds_read_b64_tr_b16 a[154:155], v63 offset:22528\n\t"
      "s_waitcnt lgkmcnt(13)\n\t"
      "v_mfma_f32_32x32x16_bf16 v[96:111], a[12:15], v[144:147], v[96:111]\n\t"
      "ds_read_b64_tr_b16 v[140:141], v191 offset:49152\n\t"
      "ds_read_b64_tr_b16 v[142:143], v191 offset:49408\n\t"
      "ds_read_b64_tr_b16 a[156:157], v63 offset:26624\n\t"
      "buffer_load_dwordx4 v34, s[32:35], s55 offen offset:3072 lds\n\t"
      "s_add_u32 m0, s52, 0x18800\n\t"
      "s_add_u32 s55, s82, 0x110000\n\t"
      "s_waitcnt lgkmcnt(13)\n\t"
      "v_mfma_f32_32x32x16_bf16 v[112:127], a[12:15], v[148:151], v[112:127]\n\t"
      "ds_read_b64_tr_b16 v[144:145], v192 offset:49152\n\t"
      "ds_read_b64_tr_b16 v[146:147], v192 offset:49408\n\t"
      "ds_read_b64_tr_b16 a[158:159], v63 offset:30720\n\t"
      "s_waitcnt lgkmcnt(13)\n\t"
      "v_mfma_f32_32x32x16_bf16 v[64:79], a[16:19], v[152:155], v[64:79]\n\t"
      "ds_read_b64_tr_b16 v[148:149], v193 offset:49152\n\t"
      "ds_read_b64_tr_b16 v[150:151], v193 offset:49408\n\t"
      "buffer_load_dwordx4 v196, s[8:11], s55 offen sc1 lds\n\t"
      "v_mfma_f32_4x4x4_16b_bf16 v[128:131], v[152:153], v[152:153], v[128:131]\n\t"
      "v_mfma_f32_4x4x4_16b_bf16 v[132:135], v[154:155], v[154:155], v[132:135]\n\t"
      "s_waitcnt lgkmcnt(12)\n\t"
      "v_mfma_f32_32x32x16_bf16 v[80:95], a[16:19], v[156:159], v[80:95]\n\t"
      "ds_read_b64_tr_b16 v[152:153], v194 offset:49152\n\t"
      "ds_read_b64_tr_b16 v[154:155], v194 offset:49408\n\t"
      "s_waitcnt lgkmcnt(11)\n\t"
      "v_mfma_f32_32x32x16_bf16 v[96:111], a[16:19], v[136:139], v[96:111]\n\t"
      "ds_read_b64_tr_b16 v[156:157], v195 offset:49152\n\t"
      "ds_read_b64_tr_b16 v[158:159], v195 offset:49408\n\t"
      "buffer_load_dwordx4 v197, s[8:11], s55 offen offset:1024 sc1 lds\n\t"
      "s_waitcnt lgkmcnt(10)\n\t"
      "v_mfma_f32_32x32x16_bf16 v[112:127], a[16:19], v[140:143], v[112:127]\n\t"
      "ds_read_b64_tr_b16 v[136:137], v188 offset:57344\n\t"
      "ds_read_b64_tr_b16 v[138:139], v188 offset:57600\n\t"
      "s_waitcnt lgkmcnt(9)\n\t"
      "v_mfma_f32_32x32x16_bf16 v[64:79], a[20:23], v[144:147], v[64:79]\n\t"
      "ds_read_b64_tr_b16 v[140:141], v189 offset:57344\n\t"
      "ds_read_b64_tr_b16 v[142:143], v189 offset:57600\n\t"
      "buffer_load_dwordx4 v198, s[8:11], s55 offen offset:2048 sc1 lds\n\t"
      "s_waitcnt lgkmcnt(8)\n\t"
      "v_mfma_f32_32x32x16_bf16 v[80:95], a[20:23], v[148:151], v[80:95]\n\t"
      "ds_read_b64_tr_b16 v[144:145], v190 offset:57344\n\t"
      "ds_read_b64_tr_b16 v[146:147], v190 offset:57600\n\t"
      "s_waitcnt lgkmcnt(8)\n\t"
      "v_mfma_f32_32x32x16_bf16 v[96:111], a[20:23], v[152:155], v[96:111]\n\t"
      "ds_read_b64_tr_b16 v[148:149], v191 offset:57344\n\t"
      "ds_read_b64_tr_b16 v[150:151], v191 offset:57600\n\t"
      "buffer_load_dwordx4 v199, s[8:11], s55 offen offset:3072 sc1 lds\n\t"
      "s_add_u32 m0, s52, 0x1c800\n\t"
      "s_add_u32 s55, s82, 0x154000\n\t"
      "s_waitcnt lgkmcnt(8)\n\t"
      "v_mfma_f32_32x32x16_bf16 v[112:127], a[20:23], v[156:159], v[112:127]\n\t"
      "ds_read_b64_tr_b16 v[152:153], v192 offset:57344\n\t"
      "ds_read_b64_tr_b16 v[154:155], v192 offset:57600\n\t"
      "s_waitcnt lgkmcnt(8)\n\t"
      "v_mfma_f32_32x32x16_bf16 v[64:79], a[24:27], v[136:139], v[64:79]\n\t"
      "ds_read_b64_tr_b16 v[156:157], v193 offset:57344\n\t"
      "ds_read_b64_tr_b16 v[158:159], v193 offset:57600\n\t"
      "buffer_load_dwordx4 v196, s[8:11], s55 offen sc1 lds\n\t"
      "v_mfma_f32_4x4x4_16b_bf16 v[128:131], v[136:137], v[136:137], v[128:131]\n\t"
      "v_mfma_f32_4x4x4_16b_bf16 v[132:135], v[138:139], v[138:139], v[132:135]\n\t"
      "s_waitcnt lgkmcnt(8)\n\t"
      "v_mfma_f32_32x32x16_bf16 v[80:95], a[24:27], v[140:143], v[80:95]\n\t"
      "ds_read_b64_tr_b16 v[136:137], v194 offset:57344\n\t"
      "ds_read_b64_tr_b16 v[138:139], v194 offset:57600\n\t"
      "s_waitcnt lgkmcnt(8)\n\t"
      "v_mfma_f32_32x32x16_bf16 v[96:111], a[24:27], v[144:147], v[96:111]\n\t"
      "s_waitcnt vmcnt(13)\n\t"
      "s_barrier\n\t"
      "ds_read_b64_tr_b16 v[140:141], v195 offset:57344\n\t"
      "ds_read_b64_tr_b16 v[142:143], v195 offset:57600\n\t"
      "buffer_load_dwordx4 v197, s[8:11], s55 offen offset:1024 sc1 lds\n\t"
      "s_waitcnt lgkmcnt(8)\n\t"
      "v_mfma_f32_32x32x16_bf16 v[112:127], a[24:27], v[148:151], v[112:127]\n\t"
      "v_add_u32 v188, 0x10000, v188\n\t"
      "ds_read_b64_tr_b16 v[144:145], v188 offset:0\n\t"
      "ds_read_b64_tr_b16 v[146:147], v188 offset:256\n\t"
      "s_waitcnt lgkmcnt(8)\n\t"
      "v_mfma_f32_32x32x16_bf16 v[64:79], a[28:31], v[152:155], v[64:79]\n\t"
      "v_add_u32 v189, 0x10000, v189\n\t"
      "ds_read_b64_tr_b16 v[148:149], v189 offset:0\n\t"
      "ds_read_b64_tr_b16 v[150:151], v189 offset:256\n\t"
      "buffer_load_dwordx4 v198, s[8:11], s55 offen offset:2048 sc1 lds\n\t"
      "s_waitcnt lgkmcnt(8)\n\t"
      "v_mfma_f32_32x32x16_bf16 v[80:95], a[28:31], v[156:159], v[80:95]\n\t"
      "v_add_u32 v190, 0x10000, v190\n\t"
      "ds_read_b64_tr_b16 v[152:153], v190 offset:0\n\t"
      "ds_read_b64_tr_b16 v[154:155], v190 offset:256\n\t"
      "s_waitcnt lgkmcnt(8)\n\t"
      "v_mfma_f32_32x32x16_bf16 v[96:111], a[28:31], v[136:139], v[96:111]\n\t"
      "v_add_u32 v191, 0x10000, v191\n\t"
      "ds_read_b64_tr_b16 v[156:157], v191 offset:0\n\t"
      "ds_read_b64_tr_b16 v[158:159], v191 offset:256\n\t"
      "buffer_load_dwordx4 v199, s[8:11], s55 offen offset:3072 sc1 lds\n\t"
      "s_add_u32 m0, s52, 0x8800\n\t"
      "s_add_u32 s55, s54, 0x30000\n\t"
      "s_waitcnt lgkmcnt(8)\n\t"
      "v_mfma_f32_32x32x16_bf16 v[112:127], a[28:31], v[140:143], v[112:127]\n\t"
      "v_add_u32 v192, 0x10000, v192\n\t"
      "ds_read_b64_tr_b16 v[136:137], v192 offset:0\n\t"
      "ds_read_b64_tr_b16 v[138:139], v192 offset:256\n\t"
      "s_waitcnt lgkmcnt(8)\n\t"
      "v_mfma_f32_32x32x16_bf16 v[64:79], a[32:35], v[144:147], v[64:79]\n\t"
      "v_add_u32 v193, 0x10000, v193\n\t"
      "ds_read_b64_tr_b16 v[140:141], v193 offset:0\n\t"
      "ds_read_b64_tr_b16 v[142:143], v193 offset:256\n\t"
      "buffer_load_dwordx4 v34, s[32:35], s55 offen lds\n\t"
      "s_add_u32 s55, s54, 0x10fc00\n\t"
      "v_mfma_f32_4x4x4_16b_bf16 v[128:131], v[144:145], v[144:145], v[128:131]\n\t"
      "v_mfma_f32_4x4x4_16b_bf16 v[132:135], v[146:147], v[146:147], v[132:135]\n\t"
      "s_waitcnt lgkmcnt(8)\n\t"
      "v_mfma_f32_32x32x16_bf16 v[80:95], a[32:35], v[148:151], v[80:95]\n\t"
      "v_add_u32 v194, 0x10000, v194\n\t"
      "ds_read_b64_tr_b16 v[144:145], v194 offset:0\n\t"
      "ds_read_b64_tr_b16 v[146:147], v194 offset:256\n\t"
      "s_waitcnt lgkmcnt(8)\n\t"
      "v_mfma_f32_32x32x16_bf16 v[96:111], a[32:35], v[152:155], v[96:111]\n\t"
      "v_add_u32 v195, 0x10000, v195\n\t"
      "ds_read_b64_tr_b16 v[148:149], v195 offset:0\n\t"
      "ds_read_b64_tr_b16 v[150:151], v195 offset:256\n\t"
      "buffer_load_dwordx4 v34, s[32:35], s55 offen offset:1024 lds\n\t"
      "s_add_u32 s55, s54, 0x37800\n\t"
      "s_waitcnt lgkmcnt(8)\n\t"
      "v_mfma_f32_32x32x16_bf16 v[112:127], a[32:35], v[156:159], v[112:127]\n\t"
      "ds_read_b64_tr_b16 v[152:153], v188 offset:8192\n\t"
      "ds_read_b64_tr_b16 v[154:155], v188 offset:8448\n\t"
      "s_waitcnt lgkmcnt(8)\n\t"
      "v_mfma_f32_32x32x16_bf16 v[64:79], a[36:39], v[136:139], v[64:79]\n\t"
      "ds_read_b64_tr_b16 v[156:157], v189 offset:8192\n\t"
      "ds_read_b64_tr_b16 v[158:159], v189 offset:8448\n\t"
      "buffer_load_dwordx4 v34, s[32:35], s55 offen offset:2048 lds\n\t"
      "s_add_u32 s55, s54, 0x117400\n\t"
      "s_waitcnt lgkmcnt(8)\n\t"
      "v_mfma_f32_32x32x16_bf16 v[80:95], a[36:39], v[140:143], v[80:95]\n\t"
      "ds_read_b64_tr_b16 v[136:137], v190 offset:8192\n\t"
      "ds_read_b64_tr_b16 v[138:139], v190 offset:8448\n\t"
      "s_waitcnt lgkmcnt(8)\n\t"
      "v_mfma_f32_32x32x16_bf16 v[96:111], a[36:39], v[144:147], v[96:111]\n\t"
      "ds_read_b64_tr_b16 v[140:141], v191 offset:8192\n\t"
      "ds_read_b64_tr_b16 v[142:143], v191 offset:8448\n\t"
      "buffer_load_dwordx4 v34, s[32:35], s55 offen offset:3072 lds\n\t"
      "s_add_u32 m0, s52, 0x800\n\t"
      "s_add_u32 s55, s82, 0x198000\n\t"
      "s_waitcnt lgkmcnt(8)\n\t"
      "v_mfma_f32_32x32x16_bf16 v[112:127], a[36:39], v[148:151], v[112:127]\n\t"
      "ds_read_b64_tr_b16 v[144:145], v192 offset:8192\n\t"
      "ds_read_b64_tr_b16 v[146:147], v192 offset:8448\n\t"
      "s_waitcnt lgkmcnt(8)\n\t"
      "v_mfma_f32_32x32x16_bf16 v[64:79], a[40:43], v[152:155], v[64:79]\n\t"
      "ds_read_b64_tr_b16 v[148:149], v193 offset:8192\n\t"
      "ds_read_b64_tr_b16 v[150:151], v193 offset:8448\n\t"
      "buffer_load_dwordx4 v196, s[8:11], s55 offen sc1 lds\n\t"
      "v_mfma_f32_4x4x4_16b_bf16 v[128:131], v[152:153], v[152:153], v[128:131]\n\t"
      "v_mfma_f32_4x4x4_16b_bf16 v[132:135], v[154:155], v[154:155], v[132:135]\n\t"
      "s_waitcnt lgkmcnt(8)\n\t"
      "v_mfma_f32_32x32x16_bf16 v[80:95], a[40:43], v[156:159], v[80:95]\n\t"
      "ds_read_b64_tr_b16 v[152:153], v194 offset:8192\n\t"
      "ds_read_b64_tr_b16 v[154:155], v194 offset:8448\n\t"
      "s_waitcnt lgkmcnt(8)\n\t"
      "v_mfma_f32_32x32x16_bf16 v[96:111], a[40:43], v[136:139], v[96:111]\n\t"
      "s_waitcnt vmcnt(17)\n\t"
      "s_barrier\n\t"
      "ds_read_b64_tr_b16 v[156:157], v195 offset:8192\n\t"
      "ds_read_b64_tr_b16 v[158:159], v195 offset:8448\n\t"
      "buffer_load_dwordx4 v197, s[8:11], s55 offen offset:1024 sc1 lds\n\t"
      "s_waitcnt lgkmcnt(8)\n\t"
      "v_mfma_f32_32x32x16_bf16 v[112:127], a[40:43], v[140:143], v[112:127]\n\t"
      "ds_read_b64_tr_b16 v[136:137], v188 offset:16384\n\t"
      "ds_read_b64_tr_b16 v[138:139], v188 offset:16640\n\t"
      "s_waitcnt lgkmcnt(8)\n\t"
      "v_mfma_f32_32x32x16_bf16 v[64:79], a[44:47], v[144:147], v[64:79]\n\t"
      "ds_read_b64_tr_b16 v[140:141], v189 offset:16384\n\t"
      "ds_read_b64_tr_b16 v[142:143], v189 offset:16640\n\t"
      "buffer_load_dwordx4 v198, s[8:11], s55 offen offset:2048 sc1 lds\n\t"
      "s_waitcnt lgkmcnt(8)\n\t"
      "v_mfma_f32_32x32x16_bf16 v[80:95], a[44:47], v[148:151], v[80:95]\n\t"
      "ds_read_b64_tr_b16 v[144:145], v190 offset:16384\n\t"
      "ds_read_b64_tr_b16 v[146:147], v190 offset:16640\n\t"
      "s_waitcnt lgkmcnt(8)\n\t"
      "v_mfma_f32_32x32x16_bf16 v[96:111], a[44:47], v[152:155], v[96:111]\n\t"
      "ds_read_b64_tr_b16 v[148:149], v191 offset:16384\n\t"
      "ds_read_b64_tr_b16 v[150:151], v191 offset:16640\n\t"
      "buffer_load_dwordx4 v199, s[8:11], s55 offen offset:3072 sc1 lds\n\t"
      "s_add_u32 m0, s52, 0xc800\n\t"
      "s_add_u32 s55, s54, 0x40000\n\t"
      "s_waitcnt lgkmcnt(8)\n\t"
      "v_mfma_f32_32x32x16_bf16 v[112:127], a[44:47], v[156:159], v[112:127]\n\t"
      "ds_read_b64_tr_b16 v[152:153], v192 offset:16384\n\t"
      "ds_read_b64_tr_b16 v[154:155], v192 offset:16640\n\t"
      "s_waitcnt lgkmcnt(8)\n\t"
      "v_mfma_f32_32x32x16_bf16 v[64:79], a[48:51], v[136:139], v[64:79]\n\t"
      "ds_read_b64_tr_b16 v[156:157], v193 offset:16384\n\t"
      "ds_read_b64_tr_b16 v[158:159], v193 offset:16640\n\t"
      "buffer_load_dwordx4 v34, s[32:35], s55 offen lds\n\t"
      "s_add_u32 s55, s54, 0x11fc00\n\t"
      "v_mfma_f32_4x4x4_16b_bf16 v[128:131], v[136:137], v[136:137], v[128:131]\n\t"
      "v_mfma_f32_4x4x4_16b_bf16 v[132:135], v[138:139], v[138:139], v[132:135]\n\t"
      "s_waitcnt lgkmcnt(8)\n\t"
      "v_mfma_f32_32x32x16_bf16 v[80:95], a[48:51], v[140:143], v[80:95]\n\t"
      "ds_read_b64_tr_b16 v[136:137], v194 offset:16384\n\t"
      "ds_read_b64_tr_b16 v[138:139], v194 offset:16640\n\t"
      "s_waitcnt lgkmcnt(8)\n\t"
      "v_mfma_f32_32x32x16_bf16 v[96:111], a[48:51], v[144:147], v[96:111]\n\t"
      "ds_read_b64_tr_b16 v[140:141], v195 offset:16384\n\t"
      "ds_read_b64_tr_b16 v[142:143], v195 offset:16640\n\t"
      "buffer_load_dwordx4 v34, s[32:35], s55 offen offset:1024 lds\n\t"
      "s_add_u32 s55, s54, 0x47800\n\t"
      "s_waitcnt lgkmcnt(8)\n\t"
      "v_mfma_f32_32x32x16_bf16 v[112:127], a[48:51], v[148:151], v[112:127]\n\t"
      "ds_read_b64_tr_b16 v[144:145], v188 offset:24576\n\t"
      "ds_read_b64_tr_b16 v[146:147], v188 offset:24832\n\t"
      "s_waitcnt lgkmcnt(8)\n\t"
      "v_mfma_f32_32x32x16_bf16 v[64:79], a[52:55], v[152:155], v[64:79]\n\t"
      "ds_read_b64_tr_b16 v[148:149], v189 offset:24576\n\t"
      "ds_read_b64_tr_b16 v[150:151], v189 offset:24832\n\t"
      "buffer_load_dwordx4 v34, s[32:35], s55 offen offset:2048 lds\n\t"
      "s_add_u32 s55, s54, 0x127400\n\t"
      "s_waitcnt lgkmcnt(8)\n\t"
      "v_mfma_f32_32x32x16_bf16 v[80:95], a[52:55], v[156:159], v[80:95]\n\t"
      "ds_read_b64_tr_b16 v[152:153], v190 offset:24576\n\t"
      "ds_read_b64_tr_b16 v[154:155], v190 offset:24832\n\t"
      "s_waitcnt lgkmcnt(8)\n\t"
      "v_mfma_f32_32x32x16_bf16 v[96:111], a[52:55], v[136:139], v[96:111]\n\t"
      "ds_read_b64_tr_b16 v[156:157], v191 offset:24576\n\t"
      "ds_read_b64_tr_b16 v[158:159], v191 offset:24832\n\t"
      "buffer_load_dwordx4 v34, s[32:35], s55 offen offset:3072 lds\n\t"
      "s_add_u32 m0, s52, 0x4800\n\t"
      "s_add_u32 s55, s82, 0x1dc000\n\t"
      "s_waitcnt lgkmcnt(8)\n\t"
      "v_mfma_f32_32x32x16_bf16 v[112:127], a[52:55], v[140:143], v[112:127]\n\t"
      "ds_read_b64_tr_b16 v[136:137], v192 offset:24576\n\t"
      "ds_read_b64_tr_b16 v[138:139], v192 offset:24832\n\t"
      "s_waitcnt lgkmcnt(8)\n\t"
      "v_mfma_f32_32x32x16_bf16 v[64:79], a[56:59], v[144:147], v[64:79]\n\t"
      "ds_read_b64_tr_b16 v[140:141], v193 offset:24576\n\t"
      "ds_read_b64_tr_b16 v[142:143], v193 offset:24832\n\t"
      "buffer_load_dwordx4 v196, s[8:11], s55 offen sc1 lds\n\t"
      "v_mfma_f32_4x4x4_16b_bf16 v[128:131], v[144:145], v[144:145], v[128:131]\n\t"
      "v_mfma_f32_4x4x4_16b_bf16 v[132:135], v[146:147], v[146:147], v[132:135]\n\t"
      "s_waitcnt lgkmcnt(8)\n\t"
      "v_mfma_f32_32x32x16_bf16 v[80:95], a[56:59], v[148:151], v[80:95]\n\t"
      "ds_read_b64_tr_b16 v[144:145], v194 offset:24576\n\t"
      "ds_read_b64_tr_b16 v[146:147], v194 offset:24832\n\t"
      "s_waitcnt lgkmcnt(8)\n\t"
      "v_mfma_f32_32x32x16_bf16 v[96:111], a[56:59], v[152:155], v[96:111]\n\t"
      "s_waitcnt vmcnt(17)\n\t"
      "s_barrier\n\t"
      "ds_read_b64_tr_b16 v[148:149], v195 offset:24576\n\t"
      "ds_read_b64_tr_b16 v[150:151], v195 offset:24832\n\t"
      "buffer_load_dwordx4 v197, s[8:11], s55 offen offset:1024 sc1 lds\n\t"
      "s_waitcnt lgkmcnt(8)\n\t"
      "v_mfma_f32_32x32x16_bf16 v[112:127], a[56:59], v[156:159], v[112:127]\n\t"
      "ds_read_b64_tr_b16 v[152:153], v188 offset:32768\n\t"
      "ds_read_b64_tr_b16 v[154:155], v188 offset:33024\n\t"
      "s_waitcnt lgkmcnt(8)\n\t"
      "v_mfma_f32_32x32x16_bf16 v[64:79], a[60:63], v[136:139], v[64:79]\n\t"
      "ds_read_b64_tr_b16 v[156:157], v189 offset:32768\n\t"
      "ds_read_b64_tr_b16 v[158:159], v189 offset:33024\n\t"
      "buffer_load_dwordx4 v198, s[8:11], s55 offen offset:2048 sc1 lds\n\t"
      "s_waitcnt lgkmcnt(8)\n\t"
      "v_mfma_f32_32x32x16_bf16 v[80:95], a[60:63], v[140:143], v[80:95]\n\t"
      "ds_read_b64_tr_b16 v[136:137], v190 offset:32768\n\t"
      "ds_read_b64_tr_b16 v[138:139], v190 offset:33024\n\t"
      "s_waitcnt lgkmcnt(8)\n\t"
      "v_mfma_f32_32x32x16_bf16 v[96:111], a[60:63], v[144:147], v[96:111]\n\t"
      "ds_read_b64_tr_b16 v[140:141], v191 offset:32768\n\t"
      "ds_read_b64_tr_b16 v[142:143], v191 offset:33024\n\t"
      "buffer_load_dwordx4 v199, s[8:11], s55 offen offset:3072 sc1 lds\n\t"
      "s_add_u32 m0, s52, 0x10800\n\t"
      "s_add_u32 s55, s54, 0x50000\n\t"
      "s_waitcnt lgkmcnt(8)\n\t"
      "v_mfma_f32_32x32x16_bf16 v[112:127], a[60:63], v[148:151], v[112:127]\n\t"
      "ds_read_b64_tr_b16 v[144:145], v192 offset:32768\n\t"
      "ds_read_b64_tr_b16 v[146:147], v192 offset:33024\n\t"
      "s_waitcnt lgkmcnt(8)\n\t"
      "v_mfma_f32_32x32x16_bf16 v[64:79], a[64:67], v[152:155], v[64:79]\n\t"
      "ds_read_b64_tr_b16 v[148:149], v193 offset:32768\n\t"
      "ds_read_b64_tr_b16 v[150:151], v193 offset:33024\n\t"
      "buffer_load_dwordx4 v34, s[32:35], s55 offen lds\n\t"
      "s_add_u32 s55, s54, 0x12fc00\n\t"
      "v_mfma_f32_4x4x4_16b_bf16 v[128:131], v[152:153], v[152:153], v[128:131]\n\t"
      "v_mfma_f32_4x4x4_16b_bf16 v[132:135], v[154:155], v[154:155], v[132:135]\n\t"
      "s_waitcnt lgkmcnt(8)\n\t"
      "v_mfma_f32_32x32x16_bf16 v[80:95], a[64:67], v[156:159], v[80:95]\n\t"
      "ds_read_b64_tr_b16 v[152:153], v194 offset:32768\n\t"
      "ds_read_b64_tr_b16 v[154:155], v194 offset:33024\n\t"
      "s_waitcnt lgkmcnt(8)\n\t"
      "v_mfma_f32_32x32x16_bf16 v[96:111], a[64:67], v[136:139], v[96:111]\n\t"
      "ds_read_b64_tr_b16 v[156:157], v195 offset:32768\n\t"
      "ds_read_b64_tr_b16 v[158:159], v195 offset:33024\n\t"
      "buffer_load_dwordx4 v34, s[32:35], s55 offen offset:1024 lds\n\t"
      "s_add_u32 s55, s54, 0x57800\n\t"
      "s_waitcnt lgkmcnt(8)\n\t"
      "v_mfma_f32_32x32x16_bf16 v[112:127], a[64:67], v[140:143], v[112:127]\n\t"
      "ds_read_b64_tr_b16 v[136:137], v188 offset:40960\n\t"
      "ds_read_b64_tr_b16 v[138:139], v188 offset:41216\n\t"
      "s_waitcnt lgkmcnt(8)\n\t"
      "v_mfma_f32_32x32x16_bf16 v[64:79], a[68:71], v[144:147], v[64:79]\n\t"
      "ds_read_b64_tr_b16 v[140:141], v189 offset:40960\n\t"
      "ds_read_b64_tr_b16 v[142:143], v189 offset:41216\n\t"
      "buffer_load_dwordx4 v34, s[32:35], s55 offen offset:2048 lds\n\t"
      "s_add_u32 s55, s54, 0x137400\n\t"
      "s_waitcnt lgkmcnt(8)\n\t"
      "v_mfma_f32_32x32x16_bf16 v[80:95], a[68:71], v[148:151], v[80:95]\n\t"
      "ds_read_b64_tr_b16 v[144:145], v190 offset:40960\n\t"
      "ds_read_b64_tr_b16 v[146:147], v190 offset:41216\n\t"
      "s_waitcnt lgkmcnt(8)\n\t"
      "v_mfma_f32_32x32x16_bf16 v[96:111], a[68:71], v[152:155], v[96:111]\n\t"
      "ds_read_b64_tr_b16 v[148:149], v191 offset:40960\n\t"
      "ds_read_b64_tr_b16 v[150:151], v191 offset:41216\n\t"
      "buffer_load_dwordx4 v34, s[32:35], s55 offen offset:3072 lds\n\t"
      "s_waitcnt lgkmcnt(8)\n\t"
      "v_mfma_f32_32x32x16_bf16 v[112:127], a[68:71], v[156:159], v[112:127]\n\t"
      "ds_read_b64_tr_b16 v[152:153], v192 offset:40960\n\t"
      "ds_read_b64_tr_b16 v[154:155], v192 offset:41216\n\t"
      "s_waitcnt lgkmcnt(8)\n\t"
      "v_mfma_f32_32x32x16_bf16 v[64:79], a[72:75], v[136:139], v[64:79]\n\t"
      "ds_read_b64_tr_b16 v[156:157], v193 offset:40960\n\t"
      "ds_read_b64_tr_b16 v[158:159], v193 offset:41216\n\t"
      "v_mfma_f32_4x4x4_16b_bf16 v[128:131], v[136:137], v[136:137], v[128:131]\n\t"
      "v_mfma_f32_4x4x4_16b_bf16 v[132:135], v[138:139], v[138:139], v[132:135]\n\t"
      "s_waitcnt lgkmcnt(8)\n\t"
      "v_mfma_f32_32x32x16_bf16 v[80:95], a[72:75], v[140:143], v[80:95]\n\t"
      "ds_read_b64_tr_b16 v[136:137], v194 offset:40960\n\t"
      "ds_read_b64_tr_b16 v[138:139], v194 offset:41216\n\t"
      "s_waitcnt lgkmcnt(8)\n\t"
      "v_mfma_f32_32x32x16_bf16 v[96:111], a[72:75], v[144:147], v[96:111]\n\t"
      "s_waitcnt vmcnt(20)\n\t"
      "s_barrier\n\t"
      "ds_read_b64_tr_b16 v[140:141], v195 offset:40960\n\t"
      "ds_read_b64_tr_b16 v[142:143], v195 offset:41216\n\t"
      "s_add_u32 m0, s52, 0x14800\n\t"
      "s_add_u32 s55, s54, 0x60000\n\t"
      "buffer_load_dwordx4 v34, s[32:35], s55 offen lds\n\t"
      "s_waitcnt lgkmcnt(8)\n\t"
      "v_mfma_f32_32x32x16_bf16 v[112:127], a[72:75], v[148:151], v[112:127]\n\t"
      "ds_read_b64_tr_b16 v[144:145], v188 offset:49152\n\t"
      "ds_read_b64_tr_b16 v[146:147], v188 offset:49408\n\t"
      "s_add_u32 s55, s54, 0x13fc00\n\t"
      "s_waitcnt lgkmcnt(8)\n\t"
      "v_mfma_f32_32x32x16_bf16 v[64:79], a[76:79], v[152:155], v[64:79]\n\t"
      "ds_read_b64_tr_b16 v[148:149], v189 offset:49152\n\t"
      "ds_read_b64_tr_b16 v[150:151], v189 offset:49408\n\t"
      "buffer_load_dwordx4 v34, s[32:35], s55 offen offset:1024 lds\n\t"
      "s_add_u32 s55, s54, 0x67800\n\t"
      "s_waitcnt lgkmcnt(8)\n\t"
      "v_mfma_f32_32x32x16_bf16 v[80:95], a[76:79], v[156:159], v[80:95]\n\t"
      "ds_read_b64_tr_b16 v[152:153], v190 offset:49152\n\t"
      "ds_read_b64_tr_b16 v[154:155], v190 offset:49408\n\t"
      "s_waitcnt lgkmcnt(8)\n\t"
      "v_mfma_f32_32x32x16_bf16 v[96:111], a[76:79], v[136:139], v[96:111]\n\t"
      "ds_read_b64_tr_b16 v[156:157], v191 offset:49152\n\t"
      "ds_read_b64_tr_b16 v[158:159], v191 offset:49408\n\t"
      "buffer_load_dwordx4 v34, s[32:35], s55 offen offset:2048 lds\n\t"
      "s_add_u32 s55, s54, 0x147400\n\t"
      "s_waitcnt lgkmcnt(8)\n\t"
      "v_mfma_f32_32x32x16_bf16 v[112:127], a[76:79], v[140:143], v[112:127]\n\t"
      "ds_read_b64_tr_b16 v[136:137], v192 offset:49152\n\t"
      "ds_read_b64_tr_b16 v[138:139], v192 offset:49408\n\t"
      "s_waitcnt lgkmcnt(8)\n\t"
      "v_mfma_f32_32x32x16_bf16 v[64:79], a[80:83], v[144:147], v[64:79]\n\t"
      "ds_read_b64_tr_b16 v[140:141], v193 offset:49152\n\t"
      "ds_read_b64_tr_b16 v[142:143], v193 offset:49408\n\t"
      "buffer_load_dwordx4 v34, s[32:35], s55 offen offset:3072 lds\n\t"
      "v_mfma_f32_4x4x4_16b_bf16 v[128:131], v[144:145], v[144:145], v[128:131]\n\t"
      "v_mfma_f32_4x4x4_16b_bf16 v[132:135], v[146:147], v[146:147], v[132:135]\n\t"
      "s_waitcnt lgkmcnt(8)\n\t"
      "v_mfma_f32_32x32x16_bf16 v[80:95], a[80:83], v[148:151], v[80:95]\n\t"
      "ds_read_b64_tr_b16 v[144:145], v194 offset:49152\n\t"
      "ds_read_b64_tr_b16 v[146:147], v194 offset:49408\n\t"
      "s_waitcnt lgkmcnt(8)\n\t"
      "v_mfma_f32_32x32x16_bf16 v[96:111], a[80:83], v[152:155], v[96:111]\n\t"
      "ds_read_b64_tr_b16 v[148:149], v195 offset:49152\n\t"
      "ds_read_b64_tr_b16 v[150:151], v195 offset:49408\n\t"
      "s_waitcnt lgkmcnt(8)\n\t"
      "v_mfma_f32_32x32x16_bf16 v[112:127], a[80:83], v[156:159], v[112:127]\n\t"
      "ds_read_b64_tr_b16 v[152:153], v188 offset:57344\n\t"
      "ds_read_b64_tr_b16 v[154:155], v188 offset:57600\n\t"
      "s_waitcnt lgkmcnt(8)\n\t"
      "v_mfma_f32_32x32x16_bf16 v[64:79], a[84:87], v[136:139], v[64:79]\n\t"
      "ds_read_b64_tr_b16 v[156:157], v189 offset:57344\n\t"
      "ds_read_b64_tr_b16 v[158:159], v189 offset:57600\n\t"
      "s_waitcnt lgkmcnt(8)\n\t"
      "v_mfma_f32_32x32x16_bf16 v[80:95], a[84:87], v[140:143], v[80:95]\n\t"
      "ds_read_b64_tr_b16 v[136:137], v190 offset:57344\n\t"
      "ds_read_b64_tr_b16 v[138:139], v190 offset:57600\n\t"
      "s_waitcnt lgkmcnt(8)\n\t"
      "v_mfma_f32_32x32x16_bf16 v[96:111], a[84:87], v[144:147], v[96:111]\n\t"
      "ds_read_b64_tr_b16 v[140:141], v191 offset:57344\n\t"
      "ds_read_b64_tr_b16 v[142:143], v191 offset:57600\n\t"
      "s_waitcnt lgkmcnt(8)\n\t"
      "v_mfma_f32_32x32x16_bf16 v[112:127], a[84:87], v[148:151], v[112:127]\n\t"
      "ds_read_b64_tr_b16 v[144:145], v192 offset:57344\n\t"
      "ds_read_b64_tr_b16 v[146:147], v192 offset:57600\n\t"
      "s_waitcnt lgkmcnt(8)\n\t"
      "v_mfma_f32_32x32x16_bf16 v[64:79], a[88:91], v[152:155], v[64:79]\n\t"
      "ds_read_b64_tr_b16 v[148:149], v193 offset:57344\n\t"
      "ds_read_b64_tr_b16 v[150:151], v193 offset:57600\n\t"
      "v_mfma_f32_4x4x4_16b_bf16 v[128:131], v[152:153], v[152:153], v[128:131]\n\t"
      "v_mfma_f32_4x4x4_16b_bf16 v[132:135], v[154:155], v[154:155], v[132:135]\n\t"
      "s_waitcnt lgkmcnt(8)\n\t"
      "v_mfma_f32_32x32x16_bf16 v[80:95], a[88:91], v[156:159], v[80:95]\n\t"
      "ds_read_b64_tr_b16 v[152:153], v194 offset:57344\n\t"
      "ds_read_b64_tr_b16 v[154:155], v194 offset:57600\n\t"
      "s_waitcnt lgkmcnt(8)\n\t"
      "v_mfma_f32_32x32x16_bf16 v[96:111], a[88:91], v[136:139], v[96:111]\n\t"
      "s_waitcnt vmcnt(16)\n\t"
      "s_barrier\n\t"
      "ds_read_b64_tr_b16 v[156:157], v195 offset:57344\n\t"
      "ds_read_b64_tr_b16 v[158:159], v195 offset:57600\n\t"
      "s_add_u32 m0, s52, 0x18800\n\t"
      "s_add_u32 s55, s54, 0x70000\n\t"
      "buffer_load_dwordx4 v34, s[32:35], s55 offen lds\n\t"
      "s_waitcnt lgkmcnt(8)\n\t"
      "v_mfma_f32_32x32x16_bf16 v[112:127], a[88:91], v[140:143], v[112:127]\n\t"
      "v_subrev_u32 v188, 0x10000, v188\n\t"
      "ds_read_b64_tr_b16 v[136:137], v188 offset:0\n\t"
      "ds_read_b64_tr_b16 v[138:139], v188 offset:256\n\t"
      "s_add_u32 s55, s54, 0x14fc00\n\t"
      "s_waitcnt lgkmcnt(8)\n\t"
      "v_mfma_f32_32x32x16_bf16 v[64:79], a[92:95], v[144:147], v[64:79]\n\t"
      "v_subrev_u32 v189, 0x10000, v189\n\t"
      "ds_read_b64_tr_b16 v[140:141], v189 offset:0\n\t"
      "ds_read_b64_tr_b16 v[142:143], v189 offset:256\n\t"
      "buffer_load_dwordx4 v34, s[32:35], s55 offen offset:1024 lds\n\t"
      "s_add_u32 s55, s54, 0x77800\n\t"
      "s_waitcnt lgkmcnt(8)\n\t"
      "v_mfma_f32_32x32x16_bf16 v[80:95], a[92:95], v[148:151], v[80:95]\n\t"
      "v_subrev_u32 v190, 0x10000, v190\n\t"
      "ds_read_b64_tr_b16 v[144:145], v190 offset:0\n\t"
      "ds_read_b64_tr_b16 v[146:147], v190 offset:256\n\t"
      "s_waitcnt lgkmcnt(8)\n\t"
      "v_mfma_f32_32x32x16_bf16 v[96:111], a[92:95], v[152:155], v[96:111]\n\t"
      "v_subrev_u32 v191, 0x10000, v191\n\t"
      "ds_read_b64_tr_b16 v[148:149], v191 offset:0\n\t"
      "ds_read_b64_tr_b16 v[150:151], v191 offset:256\n\t"
      "buffer_load_dwordx4 v34, s[32:35], s55 offen offset:2048 lds\n\t"
      "s_add_u32 s55, s54, 0x157400\n\t"
      "s_waitcnt lgkmcnt(8)\n\t"
      "v_mfma_f32_32x32x16_bf16 v[112:127], a[92:95], v[156:159], v[112:127]\n\t"
      "v_subrev_u32 v192, 0x10000, v192\n\t"
      "ds_read_b64_tr_b16 v[152:153], v192 offset:0\n\t"
      "ds_read_b64_tr_b16 v[154:155], v192 offset:256\n\t"
      "s_waitcnt lgkmcnt(8)\n\t"
      "v_mfma_f32_32x32x16_bf16 v[64:79], a[96:99], v[136:139], v[64:79]\n\t"
      "v_subrev_u32 v193, 0x10000, v193\n\t"
      "ds_read_b64_tr_b16 v[156:157], v193 offset:0\n\t"
      "ds_read_b64_tr_b16 v[158:159], v193 offset:256\n\t"
      "buffer_load_dwordx4 v34, s[32:35], s55 offen offset:3072 lds\n\t"
      "v_mfma_f32_4x4x4_16b_bf16 v[128:131], v[136:137], v[136:137], v[128:131]\n\t"
      "v_mfma_f32_4x4x4_16b_bf16 v[132:135], v[138:139], v[138:139], v[132:135]\n\t"
      "s_waitcnt lgkmcnt(8)\n\t"
      "v_mfma_f32_32x32x16_bf16 v[80:95], a[96:99], v[140:143], v[80:95]\n\t"
      "v_subrev_u32 v194, 0x10000, v194\n\t"
      "ds_read_b64_tr_b16 v[136:137], v194 offset:0\n\t"
      "ds_read_b64_tr_b16 v[138:139], v194 offset:256\n\t"
      "s_waitcnt lgkmcnt(8)\n\t"
      "v_mfma_f32_32x32x16_bf16 v[96:111], a[96:99], v[144:147], v[96:111]\n\t"
      "v_subrev_u32 v195, 0x10000, v195\n\t"
      "ds_read_b64_tr_b16 v[140:141], v195 offset:0\n\t"
      "ds_read_b64_tr_b16 v[142:143], v195 offset:256\n\t"
      "s_waitcnt lgkmcnt(8)\n\t"
      "v_mfma_f32_32x32x16_bf16 v[112:127], a[96:99], v[148:151], v[112:127]\n\t"
      "ds_read_b64_tr_b16 v[144:145], v188 offset:8192\n\t"
      "ds_read_b64_tr_b16 v[146:147], v188 offset:8448\n\t"
      "s_waitcnt lgkmcnt(8)\n\t"
      "v_mfma_f32_32x32x16_bf16 v[64:79], a[100:103], v[152:155], v[64:79]\n\t"
      "ds_read_b64_tr_b16 v[148:149], v189 offset:8192\n\t"
      "ds_read_b64_tr_b16 v[150:151], v189 offset:8448\n\t"
      "s_waitcnt lgkmcnt(8)\n\t"
      "v_mfma_f32_32x32x16_bf16 v[80:95], a[100:103], v[156:159], v[80:95]\n\t"
      "ds_read_b64_tr_b16 v[152:153], v190 offset:8192\n\t"
      "ds_read_b64_tr_b16 v[154:155], v190 offset:8448\n\t"
      "s_waitcnt lgkmcnt(8)\n\t"
      "v_mfma_f32_32x32x16_bf16 v[96:111], a[100:103], v[136:139], v[96:111]\n\t"
      "ds_read_b64_tr_b16 v[156:157], v191 offset:8192\n\t"
      "ds_read_b64_tr_b16 v[158:159], v191 offset:8448\n\t"
      "s_waitcnt lgkmcnt(8)\n\t"
      "v_mfma_f32_32x32x16_bf16 v[112:127], a[100:103], v[140:143], v[112:127]\n\t"
      "ds_read_b64_tr_b16 v[136:137], v192 offset:8192\n\t"
      "ds_read_b64_tr_b16 v[138:139], v192 offset:8448\n\t"
      "s_waitcnt lgkmcnt(8)\n\t"
      "v_mfma_f32_32x32x16_bf16 v[64:79], a[104:107], v[144:147], v[64:79]\n\t"
      "ds_read_b64_tr_b16 v[140:141], v193 offset:8192\n\t"
      "ds_read_b64_tr_b16 v[142:143], v193 offset:8448\n\t"
      "v_mfma_f32_4x4x4_16b_bf16 v[128:131], v[144:145], v[144:145], v[128:131]\n\t"
      "v_mfma_f32_4x4x4_16b_bf16 v[132:135], v[146:147], v[146:147], v[132:135]\n\t"
      "s_waitcnt lgkmcnt(8)\n\t"
      "v_mfma_f32_32x32x16_bf16 v[80:95], a[104:107], v[148:151], v[80:95]\n\t"
      "ds_read_b64_tr_b16 v[144:145], v194 offset:8192\n\t"
      "ds_read_b64_tr_b16 v[146:147], v194 offset:8448\n\t"
      "s_waitcnt lgkmcnt(8)\n\t"
      "v_mfma_f32_32x32x16_bf16 v[96:111], a[104:107], v[152:155], v[96:111]\n\t"
      "s_waitcnt vmcnt(12)\n\t"
      "s_barrier\n\t"
      "ds_read_b64_tr_b16 v[148:149], v195 offset:8192\n\t"
      "ds_read_b64_tr_b16 v[150:151], v195 offset:8448\n\t"
      "s_add_u32 m0, s52, 0x1c800\n\t"
      "s_add_u32 s55, s54, 0x80000\n\t"
      "buffer_load_dwordx4 v34, s[32:35], s55 offen lds\n\t"
      "s_waitcnt lgkmcnt(8)\n\t"
      "v_mfma_f32_32x32x16_bf16 v[112:127], a[104:107], v[156:159], v[112:127]\n\t"
      "ds_read_b64_tr_b16 v[152:153], v188 offset:16384\n\t"
      "ds_read_b64_tr_b16 v[154:155], v188 offset:16640\n\t"
      "s_add_u32 s55, s54, 0x15fc00\n\t"
      "s_waitcnt lgkmcnt(8)\n\t"
      "v_mfma_f32_32x32x16_bf16 v[64:79], a[108:111], v[136:139], v[64:79]\n\t"
      "ds_read_b64_tr_b16 v[156:157], v189 offset:16384\n\t"
      "ds_read_b64_tr_b16 v[158:159], v189 offset:16640\n\t"
      "buffer_load_dwordx4 v34, s[32:35], s55 offen offset:1024 lds\n\t"
      "s_add_u32 s55, s54, 0x87800\n\t"
      "s_waitcnt lgkmcnt(8)\n\t"
      "v_mfma_f32_32x32x16_bf16 v[80:95], a[108:111], v[140:143], v[80:95]\n\t"
      "ds_read_b64_tr_b16 v[136:137], v190 offset:16384\n\t"
      "ds_read_b64_tr_b16 v[138:139], v190 offset:16640\n\t"
      "s_waitcnt lgkmcnt(8)\n\t"
      "v_mfma_f32_32x32x16_bf16 v[96:111], a[108:111], v[144:147], v[96:111]\n\t"
      "ds_read_b64_tr_b16 v[140:141], v191 offset:16384\n\t"
      "ds_read_b64_tr_b16 v[142:143], v191 offset:16640\n\t"
      "buffer_load_dwordx4 v34, s[32:35], s55 offen offset:2048 lds\n\t"
      "s_add_u32 s55, s54, 0x167400\n\t"
      "s_waitcnt lgkmcnt(8)\n\t"
      "v_mfma_f32_32x32x16_bf16 v[112:127], a[108:111], v[148:151], v[112:127]\n\t"
      "ds_read_b64_tr_b16 v[144:145], v192 offset:16384\n\t"
      "ds_read_b64_tr_b16 v[146:147], v192 offset:16640\n\t"
      "s_waitcnt lgkmcnt(8)\n\t"
      "v_mfma_f32_32x32x16_bf16 v[64:79], a[112:115], v[152:155], v[64:79]\n\t"
      "ds_read_b64_tr_b16 v[148:149], v193 offset:16384\n\t"
      "ds_read_b64_tr_b16 v[150:151], v193 offset:16640\n\t"
      "buffer_load_dwordx4 v34, s[32:35], s55 offen offset:3072 lds\n\t"
      "s_add_u32 m0, s83, 0x0\n\t"
      "s_add_u32 s55, s84, 0x0\n\t"
      "v_mfma_f32_4x4x4_16b_bf16 v[128:131], v[152:153], v[152:153], v[128:131]\n\t"
      "v_mfma_f32_4x4x4_16b_bf16 v[132:135], v[154:155], v[154:155], v[132:135]\n\t"
      "s_waitcnt lgkmcnt(8)\n\t"
      "v_mfma_f32_32x32x16_bf16 v[80:95], a[112:115], v[156:159], v[80:95]\n\t"
      "ds_read_b64_tr_b16 v[152:153], v194 offset:16384\n\t"
      "ds_read_b64_tr_b16 v[154:155], v194 offset:16640\n\t"
      "s_waitcnt lgkmcnt(8)\n\t"
      "v_mfma_f32_32x32x16_bf16 v[96:111], a[112:115], v[136:139], v[96:111]\n\t"
      "ds_read_b64_tr_b16 v[156:157], v195 offset:16384\n\t"
      "ds_read_b64_tr_b16 v[158:159], v195 offset:16640\n\t"
      "buffer_load_dwordx4 v36, s[8:11], s55 offen sc1 lds\n\t"
      "s_add_u32 m0, s83, 0x400\n\t"
      "s_add_u32 s55, s84, 0x8800\n\t"
      "s_waitcnt lgkmcnt(8)\n\t"
      "v_mfma_f32_32x32x16_bf16 v[112:127], a[112:115], v[140:143], v[112:127]\n\t"
      "ds_read_b64_tr_b16 v[136:137], v188 offset:24576\n\t"
      "ds_read_b64_tr_b16 v[138:139], v188 offset:24832\n\t"
      "s_waitcnt lgkmcnt(8)\n\t"
      "v_mfma_f32_32x32x16_bf16 v[64:79], a[116:119], v[144:147], v[64:79]\n\t"
      "ds_read_b64_tr_b16 v[140:141], v189 offset:24576\n\t"
      "ds_read_b64_tr_b16 v[142:143], v189 offset:24832\n\t"
      "buffer_load_dwordx4 v36, s[8:11], s55 offen sc1 lds\n\t"
      "s_waitcnt lgkmcnt(8)\n\t"
      "v_mfma_f32_32x32x16_bf16 v[80:95], a[116:119], v[148:151], v[80:95]\n\t"
      "ds_read_b64_tr_b16 v[144:145], v190 offset:24576\n\t"
      "ds_read_b64_tr_b16 v[146:147], v190 offset:24832\n\t"
      "s_waitcnt lgkmcnt(8)\n\t"
      "v_mfma_f32_32x32x16_bf16 v[96:111], a[116:119], v[152:155], v[96:111]\n\t"
      "ds_read_b64_tr_b16 v[148:149], v191 offset:24576\n\t"
      "ds_read_b64_tr_b16 v[150:151], v191 offset:24832\n\t"
      "s_waitcnt lgkmcnt(8)\n\t"
      "v_mfma_f32_32x32x16_bf16 v[112:127], a[116:119], v[156:159], v[112:127]\n\t"
      "ds_read_b64_tr_b16 v[152:153], v192 offset:24576\n\t"
      "ds_read_b64_tr_b16 v[154:155], v192 offset:24832\n\t"
      "s_waitcnt lgkmcnt(8)\n\t"
      "v_mfma_f32_32x32x16_bf16 v[64:79], a[120:123], v[136:139], v[64:79]\n\t"
      "ds_read_b64_tr_b16 v[156:157], v193 offset:24576\n\t"
      "ds_read_b64_tr_b16 v[158:159], v193 offset:24832\n\t"
      "v_mfma_f32_4x4x4_16b_bf16 v[128:131], v[136:137], v[136:137], v[128:131]\n\t"
      "v_mfma_f32_4x4x4_16b_bf16 v[132:135], v[138:139], v[138:139], v[132:135]\n\t"
      "s_waitcnt lgkmcnt(8)\n\t"
      "v_mfma_f32_32x32x16_bf16 v[80:95], a[120:123], v[140:143], v[80:95]\n\t"
      "ds_read_b64_tr_b16 v[136:137], v194 offset:24576\n\t"
      "ds_read_b64_tr_b16 v[138:139], v194 offset:24832\n\t"
      "s_waitcnt lgkmcnt(8)\n\t"
      "v_mfma_f32_32x32x16_bf16 v[96:111], a[120:123], v[144:147], v[96:111]\n\t"
      "ds_read_b64_tr_b16 v[140:141], v195 offset:24576\n\t"
      "ds_read_b64_tr_b16 v[142:143], v195 offset:24832\n\t"
      "s_waitcnt lgkmcnt(8)\n\t"
      "v_mfma_f32_32x32x16_bf16 v[112:127], a[120:123], v[148:151], v[112:127]\n\t"
      "s_waitcnt lgkmcnt(6)\n\t"
      "v_mfma_f32_32x32x16_bf16 v[64:79], a[124:127], v[152:155], v[64:79]\n\t"
      "s_waitcnt lgkmcnt(4)\n\t"
      "v_mfma_f32_32x32x16_bf16 v[80:95], a[124:127], v[156:159], v[80:95]\n\t"
      "s_waitcnt lgkmcnt(2)\n\t"
      "v_mfma_f32_32x32x16_bf16 v[96:111], a[124:127], v[136:139], v[96:111]\n\t"
      "s_waitcnt lgkmcnt(0)\n\t"
      "v_mfma_f32_32x32x16_bf16 v[112:127], a[124:127], v[140:143], v[112:127]\n\t"
      "s_cmp_lg_u32 s45, 3\n\t"
      "s_cbranch_scc1 Lp3a%=\n\t"
      "s_mov_b32 exec_hi, 0\n\t"
      "s_nop 2\n\t"
      "ds_write_b32 v38, v72 offset:512\n\t"
      "s_nop 0\n\t"
      "ds_write_b32 v39, v88 offset:512\n\t"
      "s_nop 0\n\t"
      "ds_write_b32 v40, v104 offset:512\n\t"
      "s_nop 0\n\t"
      "ds_write_b32 v41, v120 offset:512\n\t"
      "s_mov_b64 exec, -1\n\t"
      "Lp3a%=:\n\t"
      "v_add_f32 v128, v128, v132\n\t"
      "v_add_f32 v129, v129, v133\n\t"
      "v_add_f32 v130, v130, v134\n\t"
      "v_add_f32 v131, v131, v135\n\t"
      "v_cndmask_b32 v128, v128, v129, s[60:61]\n\t"
      "v_cndmask_b32 v130, v130, v131, s[60:61]\n\t"
      "v_cndmask_b32 v128, v128, v130, s[62:63]\n\t"
      "ds_write_b32 v42, v128 offset:1024\n\t"
      "s_waitcnt lgkmcnt(0)\n\t"
      "s_barrier\n\t"
      "ds_read_b32 v128, v38 offset:512\n\t"
      "ds_read_b32 v130, v39 offset:512\n\t"
      "ds_read_b32 v132, v40 offset:512\n\t"
      "ds_read_b32 v134, v41 offset:512\n\t"
      "ds_read_b32 v184, v43 offset:1024\n\t"
      "ds_read_b32 v185, v43 offset:1152\n\t"
      "ds_read_b32 v186, v43 offset:1280\n\t"
      "ds_read_b32 v187, v43 offset:1408\n\t"
      "s_waitcnt lgkmcnt(4)\n\t"
      "v_cmp_u_f32 vcc, v128, v130\n\t"
      "v_cmp_u_f32 s[88:89], v132, v134\n\t"
      "v_mul_f32 v129, 0xbb000000, v128\n\t"
      "v_mul_f32 v131, 0xbb000000, v130\n\t"
      "v_mul_f32 v133, 0xbb000000, v132\n\t"
      "v_mul_f32 v135, 0xbb000000, v134\n\t"
      "s_or_b64 vcc, vcc, s[88:89]\n\t"
      "s_cbranch_scc1 Lslow1%=\n\t"
      "Lg1ok%=:\n\t"
      "v_cvt_pk_bf16_f32 v136, v129, v131\n\t"
      "v_cvt_pk_bf16_f32 v137, v133, v135\n\t"
      "s_cmp_lg_u32 s45, 3\n\t"
      "v_lshlrev_b32 v128, 16, v136\n\t"
      "v_and_b32 v130, 0xffff0000, v136\n\t"
      "v_lshlrev_b32 v132, 16, v137\n\t"
      "v_and_b32 v134, 0xffff0000, v137\n\t"
      "v_pk_fma_f32 v[64:65], v[128:129], v[160:161], v[64:65] op_sel_hi:[0,1,1]\n\t"
      "v_pk_fma_f32 v[66:67], v[128:129], v[162:163], v[66:67] op_sel_hi:[0,1,1]\n\t"
      "v_pk_fma_f32 v[68:69], v[128:129], v[164:165], v[68:69] op_sel_hi:[0,1,1]\n\t"
      "v_pk_fma_f32 v[70:71], v[128:129], v[166:167], v[70:71] op_sel_hi:[0,1,1]\n\t"
      "v_cvt_pk_bf16_f32 v140, v64, v65\n\t"
      "v_cvt_pk_bf16_f32 v141, v66, v67\n\t"
      "v_cvt_pk_bf16_f32 v142, v68, v69\n\t"
      "v_cvt_pk_bf16_f32 v143, v70, v71\n\t"
      "v_pk_max_i16 v140, v140, 0\n\t"
      "v_pk_max_i16 v141, v141, 0\n\t"
      "v_pk_max_i16 v142, v142, 0\n\t"
      "v_pk_max_i16 v143, v143, 0\n\t"
      "buffer_store_dwordx4 v[140:143], v30, s[8:11], s77 offen sc1\n\t"
      "v_pk_fma_f32 v[80:81], v[130:131], v[160:161], v[80:81] op_sel_hi:[0,1,1]\n\t"
      "v_pk_fma_f32 v[82:83], v[130:131], v[162:163], v[82:83] op_sel_hi:[0,1,1]\n\t"
      "v_pk_fma_f32 v[84:85], v[130:131], v[164:165], v[84:85] op_sel_hi:[0,1,1]\n\t"
      "v_pk_fma_f32 v[86:87], v[130:131], v[166:167], v[86:87] op_sel_hi:[0,1,1]\n\t"
      "v_cvt_pk_bf16_f32 v144, v80, v81\n\t"
      "v_cvt_pk_bf16_f32 v145, v82, v83\n\t"
      "v_cvt_pk_bf16_f32 v146, v84, v85\n\t"
      "v_cvt_pk_bf16_f32 v147, v86, v87\n\t"
      "v_pk_max_i16 v144, v144, 0\n\t"
      "v_pk_max_i16 v145, v145, 0\n\t"
      "v_pk_max_i16 v146, v146, 0\n\t"
      "v_pk_max_i16 v147, v147, 0\n\t"
      "buffer_store_dwordx4 v[144:147], v30, s[8:11], s78 offen sc1\n\t"
      "v_pk_fma_f32 v[96:97], v[132:133], v[160:161], v[96:97] op_sel_hi:[0,1,1]\n\t"
      "v_pk_fma_f32 v[98:99], v[132:133], v[162:163], v[98:99] op_sel_hi:[0,1,1]\n\t"
      "v_pk_fma_f32 v[100:101], v[132:133], v[164:165], v[100:101] op_sel_hi:[0,1,1]\n\t"
      "v_pk_fma_f32 v[102:103], v[132:133], v[166:167], v[102:103] op_sel_hi:[0,1,1]\n\t"
      "v_cvt_pk_bf16_f32 v148, v96, v97\n\t"
      "v_cvt_pk_bf16_f32 v149, v98, v99\n\t"
      "v_cvt_pk_bf16_f32 v150, v100, v101\n\t"
      "v_cvt_pk_bf16_f32 v151, v102, v103\n\t"
      "v_pk_max_i16 v148, v148, 0\n\t"
      "v_pk_max_i16 v149, v149, 0\n\t"
      "v_pk_max_i16 v150, v150, 0\n\t"
      "v_pk_max_i16 v151, v151, 0\n\t"
      "buffer_store_dwordx4 v[148:151], v30, s[8:11], s79 offen sc1\n\t"
      "v_pk_fma_f32 v[112:113], v[134:135], v[160:161], v[112:113] op_sel_hi:[0,1,1]\n\t"
      "v_pk_fma_f32 v[114:115], v[134:135], v[162:163], v[114:115] op_sel_hi:[0,1,1]\n\t"
      "v_pk_fma_f32 v[116:117], v[134:135], v[164:165], v[116:117] op_sel_hi:[0,1,1]\n\t"
      "v_pk_fma_f32 v[118:119], v[134:135], v[166:167], v[118:119] op_sel_hi:[0,1,1]\n\t"
      "v_cvt_pk_bf16_f32 v152, v112, v113\n\t"
      "v_cvt_pk_bf16_f32 v153, v114, v115\n\t"
      "v_cvt_pk_bf16_f32 v154, v116, v117\n\t"
      "v_cvt_pk_bf16_f32 v155, v118, v119\n\t"
      "v_pk_max_i16 v152, v152, 0\n\t"
      "v_pk_max_i16 v153, v153, 0\n\t"
      "v_pk_max_i16 v154, v154, 0\n\t"
      "v_pk_max_i16 v155, v155, 0\n\t"
      "buffer_store_dwordx4 v[152:155], v30, s[8:11], s80 offen sc1\n\t"
      "s_cbranch_scc0 Lp3b%=\n\t"
      "v_pk_fma_f32 v[72:73], v[128:129], v[168:169], v[72:73] op_sel_hi:[0,1,1]\n\t"
      "v_pk_fma_f32 v[74:75], v[128:129], v[170:171], v[74:75] op_sel_hi:[0,1,1]\n\t"
      "v_pk_fma_f32 v[76:77], v[128:129], v[172:173], v[76:77] op_sel_hi:[0,1,1]\n\t"
      "v_pk_fma_f32 v[78:79], v[128:129], v[174:175], v[78:79] op_sel_hi:[0,1,1]\n\t"
      "v_cvt_pk_bf16_f32 v140, v72, v73\n\t"
      "v_cvt_pk_bf16_f32 v141, v74, v75\n\t"
      "v_cvt_pk_bf16_f32 v142, v76, v77\n\t"
      "v_cvt_pk_bf16_f32 v143, v78, v79\n\t"
      "v_pk_max_i16 v140, v140, 0\n\t"
      "v_pk_max_i16 v141, v141, 0\n\t"
      "v_pk_max_i16 v142, v142, 0\n\t"
      "v_pk_max_i16 v143, v143, 0\n\t"
      "buffer_store_dwordx4 v[140:143], v30, s[8:11], s77 offen offset:1024 sc1\n\t"
      "v_pk_fma_f32 v[88:89], v[130:131], v[168:169], v[88:89] op_sel_hi:[0,1,1]\n\t"
      "v_pk_fma_f32 v[90:91], v[130:131], v[170:171], v[90:91] op_sel_hi:[0,1,1]\n\t"
      "v_pk_fma_f32 v[92:93], v[130:131], v[172:173], v[92:93] op_sel_hi:[0,1,1]\n\t"
      "v_pk_fma_f32 v[94:95], v[130:131], v[174:175], v[94:95] op_sel_hi:[0,1,1]\n\t"
      "v_cvt_pk_bf16_f32 v144, v88, v89\n\t"
      "v_cvt_pk_bf16_f32 v145, v90, v91\n\t"
      "v_cvt_pk_bf16_f32 v146, v92, v93\n\t"
      "v_cvt_pk_bf16_f32 v147, v94, v95\n\t"
      "v_pk_max_i16 v144, v144, 0\n\t"
      "v_pk_max_i16 v145, v145, 0\n\t"
      "v_pk_max_i16 v146, v146, 0\n\t"
      "v_pk_max_i16 v147, v147, 0\n\t"
      "buffer_store_dwordx4 v[144:147], v30, s[8:11], s78 offen offset:1024 sc1\n\t"
      "v_pk_fma_f32 v[104:105], v[132:133], v[168:169], v[104:105] op_sel_hi:[0,1,1]\n\t"
      "v_pk_fma_f32 v[106:107], v[132:133], v[170:171], v[106:107] op_sel_hi:[0,1,1]\n\t"
      "v_pk_fma_f32 v[108:109], v[132:133], v[172:173], v[108:109] op_sel_hi:[0,1,1]\n\t"
      "v_pk_fma_f32 v[110:111], v[132:133], v[174:175], v[110:111] op_sel_hi:[0,1,1]\n\t"
      "v_cvt_pk_bf16_f32 v148, v104, v105\n\t"
      "v_cvt_pk_bf16_f32 v149, v106, v107\n\t"
      "v_cvt_pk_bf16_f32 v150, v108, v109\n\t"
      "v_cvt_pk_bf16_f32 v151, v110, v111\n\t"
      "v_pk_max_i16 v148, v148, 0\n\t"
      "v_pk_max_i16 v149, v149, 0\n\t"
      "v_pk_max_i16 v150, v150, 0\n\t"
      "v_pk_max_i16 v151, v151, 0\n\t"
      "buffer_store_dwordx4 v[148:151], v30, s[8:11], s79 offen offset:1024 sc1\n\t"
      "v_pk_fma_f32 v[120:121], v[134:135], v[168:169], v[120:121] op_sel_hi:[0,1,1]\n\t"
      "v_pk_fma_f32 v[122:123], v[134:135], v[170:171], v[122:123] op_sel_hi:[0,1,1]\n\t"
      "v_pk_fma_f32 v[124:125], v[134:135], v[172:173], v[124:125] op_sel_hi:[0,1,1]\n\t"
      "v_pk_fma_f32 v[126:127], v[134:135], v[174:175], v[126:127] op_sel_hi:[0,1,1]\n\t"
      "v_cvt_pk_bf16_f32 v152, v120, v121\n\t"
      "v_cvt_pk_bf16_f32 v153, v122, v123\n\t"
      "v_cvt_pk_bf16_f32 v154, v124, v125\n\t"
      "v_cvt_pk_bf16_f32 v155, v126, v127\n\t"
      "v_pk_max_i16 v152, v152, 0\n\t"
      "v_pk_max_i16 v153, v153, 0\n\t"
      "v_pk_max_i16 v154, v154, 0\n\t"
      "v_pk_max_i16 v155, v155, 0\n\t"
      "buffer_store_dwordx4 v[152:155], v30, s[8:11], s80 offen offset:1024 sc1\n\t"
      "s_memrealtime s[24:25]\n\t"
      "s_branch Lp3c%=\n\t"
      "Lp3b%=:\n\t"
      "s_memrealtime s[24:25]\n\t"
      "Lp3c%=:\n\t"
      "v_lshlrev_b32 v247, 3, v213\n\t"
      "s_lshl_b32 s68, s48, 7\n\t"
      "s_add_u32 s68, s68, 28672\n\t"
      "buffer_load_dwordx2 v[244:245], v247, s[8:11], s68 offen sc1\n\t"
      "s_add_u32 m0, s52, 0x800\n\t"
      "s_add_u32 s55, s54, 0x90000\n\t"
      "buffer_load_dwordx4 v34, s[32:35], s55 offen lds\n\t"
      "s_add_u32 s55, s54, 0x16fc00\n\t"
      "buffer_load_dwordx4 v34, s[32:35], s55 offen offset:1024 lds\n\t"
      "s_add_u32 s55, s54, 0x97800\n\t"
      "buffer_load_dwordx4 v34, s[32:35], s55 offen offset:2048 lds\n\t"
      "s_add_u32 s55, s54, 0x177400\n\t"
      "buffer_load_dwordx4 v34, s[32:35], s55 offen offset:3072 lds\n\t"
      "s_add_u32 m0, s52, 0x4800\n\t"
      "s_add_u32 s55, s54, 0xa0000\n\t"
      "buffer_load_dwordx4 v34, s[32:35], s55 offen lds\n\t"
      "s_add_u32 s55, s54, 0x17fc00\n\t"
      "buffer_load_dwordx4 v34, s[32:35], s55 offen offset:1024 lds\n\t"
      "s_add_u32 s55, s54, 0xa7800\n\t"
      "buffer_load_dwordx4 v34, s[32:35], s55 offen offset:2048 lds\n\t"
      "s_add_u32 s55, s54, 0x187400\n\t"
      "buffer_load_dwordx4 v34, s[32:35], s55 offen offset:3072 lds\n\t"
      "s_waitcnt lgkmcnt(0)\n\t"
      "v_add_f32 v184, v184, v185\n\t"
      "v_add_f32 v186, v186, v187\n\t"
      "v_add_f32 v184, v184, v186\n\t"
      "v_mul_f32 v184, 0x3b000000, v184\n\t"
      "v_fma_f32 v184, -v129, v129, v184\n\t"
      "v_max_f32 v184, 0, v184\n\t"
      "v_cvt_pk_bf16_f32 v184, v184, v184\n\t"
      "v_lshlrev_b32 v184, 16, v184\n\t"
      "v_add_f32 v184, 0x3727c5ac, v184\n\t"
      "v_cvt_pk_bf16_f32 v184, v184, v184\n\t"
      "v_lshlrev_b32 v184, 16, v184\n\t"
      "v_sqrt_f32 v184, v184\n\t"
      "s_nop 0\n\t"
      "v_cvt_pk_bf16_f32 v184, v184, v184\n\t"
      "v_lshlrev_b32 v184, 16, v184\n\t"
      "v_rcp_f32 v51, v184\n\t"
      "s_waitcnt vmcnt(17)\n\t"
      "s_cmp_lg_u32 s45, 3\n\t"
      "s_cbranch_scc1 Lp3d%=\n\t"
      "s_waitcnt vmcnt(13)\n\t"
      "Lp3d%=:\n\t"
      "s_barrier\n\t"
      "ds_read_b64_tr_b16 v[176:177], v37 offset:0\n\t"
      "ds_read_b64_tr_b16 v[178:179], v37 offset:1024\n\t"
      "ds_read_b64_tr_b16 v[180:181], v37 offset:2048\n\t"
      "ds_read_b64_tr_b16 v[182:183], v37 offset:3072\n\t"
      "v_add_u32 v63, 0x8800, v35\n\t"
      "ds_read_b64_tr_b16 a[176:177], v63 offset:0\n\t"
      "ds_read_b64_tr_b16 a[178:179], v63 offset:4096\n\t"
      "ds_read_b64_tr_b16 a[180:181], v63 offset:8192\n\t"
      "ds_read_b64_tr_b16 a[182:183], v63 offset:12288\n\t"
      "ds_read_b64_tr_b16 a[184:185], v63 offset:2048\n\t"
      "ds_read_b64_tr_b16 a[186:187], v63 offset:6144\n\t"
      "ds_read_b64_tr_b16 a[188:189], v63 offset:10240\n\t"
      "ds_read_b64_tr_b16 a[190:191], v63 offset:14336\n\t"
      "ds_read_b64_tr_b16 a[192:193], v63 offset:16384\n\t"
      "ds_read_b64_tr_b16 a[194:195], v63 offset:20480\n\t"
      "ds_read_b64_tr_b16 a[196:197], v63 offset:24576\n\t"
      "ds_read_b64_tr_b16 a[198:199], v63 offset:28672\n\t"
      "ds_read_b64_tr_b16 a[200:201], v63 offset:18432\n\t"
      "ds_read_b64_tr_b16 a[202:203], v63 offset:22528\n\t"
      "ds_read_b64_tr_b16 a[204:205], v63 offset:26624\n\t"
      "ds_read_b64_tr_b16 a[206:207], v63 offset:30720\n\t"
      "ds_read_b64_tr_b16 a[208:209], v63 offset:32768\n\t"
      "ds_read_b64_tr_b16 a[210:211], v63 offset:36864\n\t"
      "ds_read_b64_tr_b16 a[212:213], v63 offset:40960\n\t"
      "ds_read_b64_tr_b16 a[214:215], v63 offset:45056\n\t"
      "ds_read_b64_tr_b16 a[216:217], v63 offset:34816\n\t"
      "ds_read_b64_tr_b16 a[218:219], v63 offset:38912\n\t"
      "ds_read_b64_tr_b16 a[220:221], v63 offset:43008\n\t"
      "ds_read_b64_tr_b16 a[222:223], v63 offset:47104\n\t"
      "ds_read_b64_tr_b16 a[224:225], v63 offset:49152\n\t"
      "ds_read_b64_tr_b16 a[226:227], v63 offset:53248\n\t"
      "ds_read_b64_tr_b16 a[228:229], v63 offset:57344\n\t"
      "ds_read_b64_tr_b16 a[230:231], v63 offset:61440\n\t"
      "ds_read_b64_tr_b16 a[232:233], v63 offset:51200\n\t"
      "ds_read_b64_tr_b16 a[234:235], v63 offset:55296\n\t"
      "ds_read_b64_tr_b16 a[236:237], v63 offset:59392\n\t"
      "ds_read_b64_tr_b16 a[238:239], v63 offset:63488\n\t"
      "v_add_u32 v63, 0x18800, v35\n\t"
      "ds_read_b64_tr_b16 a[240:241], v63 offset:0\n\t"
      "ds_read_b64_tr_b16 a[242:243], v63 offset:4096\n\t"
      "ds_read_b64_tr_b16 a[244:245], v63 offset:8192\n\t"
      "ds_read_b64_tr_b16 a[246:247], v63 offset:12288\n\t"
      "ds_read_b64_tr_b16 a[248:249], v63 offset:2048\n\t"
      "ds_read_b64_tr_b16 a[250:251], v63 offset:6144\n\t"
      "ds_read_b64_tr_b16 a[252:253], v63 offset:10240\n\t"
      "ds_read_b64_tr_b16 a[254:255], v63 offset:14336\n\t"
      "ds_read_b64_tr_b16 a[0:1], v63 offset:16384\n\t"
      "ds_read_b64_tr_b16 a[2:3], v63 offset:20480\n\t"
      "ds_read_b64_tr_b16 a[4:5], v63 offset:24576\n\t"
      "ds_read_b64_tr_b16 a[6:7], v63 offset:28672\n\t"
      "ds_read_b64_tr_b16 a[8:9], v63 offset:18432\n\t"
      "ds_read_b64_tr_b16 a[10:11], v63 offset:22528\n\t"
      "ds_read_b64_tr_b16 a[12:13], v63 offset:26624\n\t"
      "ds_read_b64_tr_b16 a[14:15], v63 offset:30720\n\t"
      "ds_read_b64_tr_b16 a[160:161], v63 offset:32768\n\t"
      "ds_read_b64_tr_b16 a[162:163], v63 offset:36864\n\t"
      "ds_read_b64_tr_b16 a[164:165], v63 offset:40960\n\t"
      "ds_read_b64_tr_b16 a[166:167], v63 offset:45056\n\t"
      "ds_read_b64_tr_b16 a[168:169], v63 offset:34816\n\t"
      "ds_read_b64_tr_b16 a[170:171], v63 offset:38912\n\t"
      "ds_read_b64_tr_b16 a[172:173], v63 offset:43008\n\t"
      "ds_read_b64_tr_b16 a[174:175], v63 offset:47104\n\t"
      "s_waitcnt vmcnt(8)\n\t"
      "s_movk_i32 s73, 0x7fff\n\t"
      "s_branch Lt3_chk%=\n\t"
      "Lt3_again%=:\n\t"
      "s_sleep 8\n\t"
      "buffer_load_dwordx2 v[244:245], v247, s[8:11], s68 offen sc1\n\t"
      "s_waitcnt vmcnt(0)\n\t"
      "Lt3_chk%=:\n\t"
      "v_cmp_ne_u32 vcc, s53, v244\n\t"
      "v_subrev_u32 v245, s26, v245\n\t"
      "s_sub_u32 s73, s73, 1\n\t"
      "s_cselect_b64 s[88:89], 0, vcc\n\t"
      "s_cmp_lg_u64 s[88:89], 0\n\t"
      "s_cbranch_scc1 Lt3_again%=\n\t"
      "v_max_i32_dpp v245, v245, v245 row_shr:1 row_mask:0xf bank_mask:0xf\n\t"
      "s_nop 1\n\t"
      "v_max_i32_dpp v245, v245, v245 row_shr:2 row_mask:0xf bank_mask:0xf\n\t"
      "s_nop 1\n\t"
      "v_max_i32_dpp v245, v245, v245 row_shr:4 row_mask:0xf bank_mask:0xf\n\t"
      "s_nop 1\n\t"
      "v_max_i32_dpp v245, v245, v245 row_shr:8 row_mask:0xf bank_mask:0xf\n\t"
      "s_mov_b32 exec_lo, 0x80008000\n\t"
      "s_mov_b32 exec_hi, 0x80008000\n\t"
      "ds_max_i32 v246, v245\n\t"
      "s_mov_b64 exec, -1\n\t"
      "s_waitcnt lgkmcnt(0)\n\t"
      "s_barrier\n\t"
      "s_cmp_lg_u32 s45, 0\n\t"
      "s_cbranch_scc1 Lf3%=\n\t"
      "s_lshl_b32 s56, s44, 4\n\t"
      "s_add_u32 s56, s56, 131072\n\t"
      "s_mov_b64 exec, 1\n\t"
      "buffer_store_dword v242, off, s[8:11], s56 sc1\n\t"
      "s_mov_b64 exec, -1\n\t"
      "Lf3%=:\n\t"
      "s_add_u32 m0, s52, 0x18800\n\t"
      "s_add_u32 s55, s54, 0xb0000\n\t"
      "buffer_load_dwordx4 v34, s[32:35], s55 offen lds\n\t"
      "s_add_u32 s55, s54, 0x18fc00\n\t"
      "buffer_load_dwordx4 v34, s[32:35], s55 offen offset:1024 lds\n\t"
      "s_add_u32 s55, s54, 0xb7800\n\t"
      "buffer_load_dwordx4 v34, s[32:35], s55 offen offset:2048 lds\n\t"
      "s_add_u32 s55, s54, 0x197400\n\t"
      "buffer_load_dwordx4 v34, s[32:35], s55 offen offset:3072 lds\n\t"
      "s_add_u32 m0, s52, 0x1c800\n\t"
      "s_add_u32 s55, s54, 0xc0000\n\t"
      "buffer_load_dwordx4 v34, s[32:35], s55 offen lds\n\t"
      "s_add_u32 s55, s54, 0x19fc00\n\t"
      "buffer_load_dwordx4 v34, s[32:35], s55 offen offset:1024 lds\n\t"
      "s_add_u32 s55, s54, 0xc7800\n\t"
      "buffer_load_dwordx4 v34, s[32:35], s55 offen offset:2048 lds\n\t"
      "s_add_u32 s55, s54, 0x1a7400\n\t"
      "buffer_load_dwordx4 v34, s[32:35], s55 offen offset:3072 lds\n\t"
      "s_add_u32 m0, s52, 0x20800\n\t"
      "s_add_u32 s55, s54, 0xd0000\n\t"
      "buffer_load_dwordx4 v34, s[32:35], s55 offen lds\n\t"
      "s_add_u32 s55, s54, 0x1afc00\n\t"
      "buffer_load_dwordx4 v34, s[32:35], s55 offen offset:1024 lds\n\t"
      "s_add_u32 s55, s54, 0xd7800\n\t"
      "buffer_load_dwordx4 v34, s[32:35], s55 offen offset:2048 lds\n\t"
      "s_add_u32 s55, s54, 0x1b7400\n\t"
      "buffer_load_dwordx4 v34, s[32:35], s55 offen offset:3072 lds\n\t"
      "v_lshlrev_b32 v140, 16, v176\n\t"
      "v_and_b32 v141, 0xffff0000, v176\n\t"
      "v_lshlrev_b32 v142, 16, v177\n\t"
      "v_and_b32 v143, 0xffff0000, v177\n\t"
      "v_lshlrev_b32 v144, 16, v178\n\t"
      "v_and_b32 v145, 0xffff0000, v178\n\t"
      "v_lshlrev_b32 v146, 16, v179\n\t"
      "v_and_b32 v147, 0xffff0000, v179\n\t"
      "v_lshlrev_b32 v148, 16, v180\n\t"
      "v_and_b32 v149, 0xffff0000, v180\n\t"
      "v_lshlrev_b32 v150, 16, v181\n\t"
      "v_and_b32 v151, 0xffff0000, v181\n\t"
      "v_lshlrev_b32 v152, 16, v182\n\t"
      "v_and_b32 v153, 0xffff0000, v182\n\t"
      "v_lshlrev_b32 v154, 16, v183\n\t"
      "v_and_b32 v155, 0xffff0000, v183\n\t"
      "v_add_u32 v31, 0x10000, v31\n\t"
      "v_add_u32 v33, 0x10000, v33\n\t"
      "s_waitcnt vmcnt(12)\n\t"
      "s_barrier\n\t"
      "v_add_u32 v63, 0x800, v35\n\t"
      "ds_read_b64_tr_b16 a[16:17], v63 offset:0\n\t"
      "ds_read_b64_tr_b16 a[18:19], v63 offset:4096\n\t"
      "ds_read_b64_tr_b16 a[20:21], v63 offset:8192\n\t"
      "ds_read_b64_tr_b16 a[22:23], v63 offset:12288\n\t"
      "ds_read_b64_tr_b16 a[24:25], v63 offset:2048\n\t"
      "ds_read_b64_tr_b16 a[26:27], v63 offset:6144\n\t"
      "ds_read_b64_tr_b16 a[28:29], v63 offset:10240\n\t"
      "ds_read_b64_tr_b16 a[30:31], v63 offset:14336\n\t"
      "ds_read_b64_tr_b16 a[32:33], v63 offset:16384\n\t"
      "ds_read_b64_tr_b16 a[34:35], v63 offset:20480\n\t"
      "ds_read_b64_tr_b16 a[36:37], v63 offset:24576\n\t"
      "ds_read_b64_tr_b16 a[38:39], v63 offset:28672\n\t"
      "ds_read_b64_tr_b16 a[40:41], v63 offset:18432\n\t"
      "ds_read_b64_tr_b16 a[42:43], v63 offset:22528\n\t"
      "ds_read_b64_tr_b16 a[44:45], v63 offset:26624\n\t"
      "ds_read_b64_tr_b16 a[46:47], v63 offset:30720\n\t"
      "ds_read_b32 v245, v246\n\t"
      "s_waitcnt lgkmcnt(0)\n\t"
      "v_readfirstlane_b32 s56, v245\n\t"
      "s_nop 0\n\t"
      "s_min_u32 s56, s56, 400\n\t"
      "s_add_u32 s56, s56, 120\n\t"
      "Lu3%=:\n\t"
      "s_memrealtime s[88:89]\n\t"
      "s_waitcnt lgkmcnt(0)\n\t"
      "s_sub_u32 s88, s88, s24\n\t"
      "s_cmp_lt_u32 s88, s56\n\t"
      "s_cbranch_scc1 Lu3%=\n\t"
      "buffer_load_dword v64, v44, s[8:11], s86 offen sc1\n\t"
      "s_movk_i32 s73, 0x7fff\n\t"
      "s_branch Ls3_chk%=\n\t"
      "Ls3_again%=:\n\t"
      "s_sleep 2\n\t"
      "buffer_load_dword v64, v44, s[8:11], s86 offen sc1\n\t"
      "Ls3_chk%=:\n\t"
      "s_waitcnt vmcnt(0)\n\t"
      "v_cmp_ne_u32 vcc, s53, v64\n\t"
      "s_sub_u32 s73, s73, 1\n\t"
      "s_cselect_b64 s[88:89], 0, vcc\n\t"
      "s_cmp_lg_u64 s[88:89], 0\n\t"
      "s_cbranch_scc1 Ls3_again%=\n\t"
      "s_waitcnt lgkmcnt(0)\n\t"
      "s_barrier\n\t"
      "Lg2%=:\n\t"
      "s_add_u32 m0, s52, 0x800\n\t"
      "s_add_u32 s55, s85, 0x0\n\t"
      "buffer_load_dwordx4 v30, s[8:11], s55 offen sc1 lds\n\t"
      "buffer_load_dwordx4 v30, s[8:11], s55 offen offset:1024 sc1 lds\n\t"
      "buffer_load_dwordx4 v30, s[8:11], s55 offen offset:2048 sc1 lds\n\t"
      "buffer_load_dwordx4 v30, s[8:11], s55 offen offset:3072 sc1 lds\n\t"
      "s_add_u32 m0, s52, 0x4800\n\t"
      "s_add_u32 s55, s85, 0x1000\n\t"
      "buffer_load_dwordx4 v30, s[8:11], s55 offen sc1 lds\n\t"
      "buffer_load_dwordx4 v30, s[8:11], s55 offen offset:1024 sc1 lds\n\t"
      "buffer_load_dwordx4 v30, s[8:11], s55 offen offset:2048 sc1 lds\n\t"
      "buffer_load_dwordx4 v30, s[8:11], s55 offen offset:3072 sc1 lds\n\t"
      "s_add_u32 m0, s52, 0x8800\n\t"
      "s_add_u32 s55, s85, 0x2000\n\t"
      "buffer_load_dwordx4 v30, s[8:11], s55 offen sc1 lds\n\t"
      "buffer_load_dwordx4 v30, s[8:11], s55 offen offset:1024 sc1 lds\n\t"
      "buffer_load_dwordx4 v30, s[8:11], s55 offen offset:2048 sc1 lds\n\t"
      "buffer_load_dwordx4 v30, s[8:11], s55 offen offset:3072 sc1 lds\n\t"
      "s_add_u32 m0, s52, 0xc800\n\t"
      "s_add_u32 s55, s85, 0x3000\n\t"
      "buffer_load_dwordx4 v30, s[8:11], s55 offen sc1 lds\n\t"
      "buffer_load_dwordx4 v30, s[8:11], s55 offen offset:1024 sc1 lds\n\t"
      "buffer_load_dwordx4 v30, s[8:11], s55 offen offset:2048 sc1 lds\n\t"
      "buffer_load_dwordx4 v30, s[8:11], s55 offen offset:3072 sc1 lds\n\t"
      "s_add_u32 m0, s52, 0x10800\n\t"
      "s_add_u32 s55, s85, 0x4000\n\t"
      "buffer_load_dwordx4 v30, s[8:11], s55 offen sc1 lds\n\t"
      "buffer_load_dwordx4 v30, s[8:11], s55 offen offset:1024 sc1 lds\n\t"
      "buffer_load_dwordx4 v30, s[8:11], s55 offen offset:2048 sc1 lds\n\t"
      "buffer_load_dwordx4 v30, s[8:11], s55 offen offset:3072 sc1 lds\n\t"
      "s_waitcnt vmcnt(16)\n\t"
      "s_barrier\n\t"
      "ds_read_b128 v[96:99], v200 offset:0\n\t"
      "ds_read_b128 v[100:103], v200 offset:4096\n\t"
      "ds_read_b128 v[104:107], v200 offset:1024\n\t"
      "ds_read_b128 v[108:111], v200 offset:5120\n\t"
      "ds_read_b128 v[112:115], v200 offset:2048\n\t"
      "v_add_u32 v63, 0x18800, v35\n\t"
      "ds_read_b64_tr_b16 a[48:49], v63 offset:0\n\t"
      "ds_read_b64_tr_b16 a[50:51], v63 offset:4096\n\t"
      "s_add_u32 m0, s52, 0x14800\n\t"
      "s_add_u32 s55, s85, 0x5000\n\t"
      "buffer_load_dwordx4 v30, s[8:11], s55 offen sc1 lds\n\t"
      "s_waitcnt lgkmcnt(6)\n\t"
      "v_mfma_f32_32x32x16_bf16 v[64:79], a[128:131], v[96:99], 0\n\t"
      "ds_read_b128 v[116:119], v200 offset:6144\n\t"
      "ds_read_b64_tr_b16 a[52:53], v63 offset:8192\n\t"
      "ds_read_b64_tr_b16 a[54:55], v63 offset:12288\n\t"
      "s_waitcnt lgkmcnt(8)\n\t"
      "v_mfma_f32_32x32x16_bf16 v[80:95], a[128:131], v[100:103], 0\n\t"
      "ds_read_b128 v[120:123], v200 offset:3072\n\t"
      "ds_read_b64_tr_b16 a[56:57], v63 offset:2048\n\t"
      "ds_read_b64_tr_b16 a[58:59], v63 offset:6144\n\t"
      "buffer_load_dwordx4 v30, s[8:11], s55 offen offset:1024 sc1 lds\n\t"
      "s_waitcnt lgkmcnt(10)\n\t"
      "v_mfma_f32_32x32x16_bf16 v[64:79], a[132:135], v[104:107], v[64:79]\n\t"
      "s_waitcnt vmcnt(14)\n\t"
      "s_barrier\n\t"
      "ds_read_b128 v[124:127], v200 offset:7168\n\t"
      "ds_read_b64_tr_b16 a[60:61], v63 offset:10240\n\t"
      "ds_read_b64_tr_b16 a[62:63], v63 offset:14336\n\t"
      "s_waitcnt lgkmcnt(12)\n\t"
      "v_mfma_f32_32x32x16_bf16 v[80:95], a[132:135], v[108:111], v[80:95]\n\t"
      "ds_read_b128 v[96:99], v200 offset:16384\n\t"
      "ds_read_b64_tr_b16 a[64:65], v63 offset:16384\n\t"
      "ds_read_b64_tr_b16 a[66:67], v63 offset:20480\n\t"
      "buffer_load_dwordx4 v30, s[8:11], s55 offen offset:2048 sc1 lds\n\t"
      "s_waitcnt lgkmcnt(14)\n\t"
      "v_mfma_f32_32x32x16_bf16 v[64:79], a[136:139], v[112:115], v[64:79]\n\t"
      "ds_read_b128 v[100:103], v200 offset:20480\n\t"
      "ds_read_b64_tr_b16 a[68:69], v63 offset:24576\n\t"
      "ds_read_b64_tr_b16 a[70:71], v63 offset:28672\n\t"
      "s_waitcnt lgkmcnt(14)\n\t"
      "v_mfma_f32_32x32x16_bf16 v[80:95], a[136:139], v[116:119], v[80:95]\n\t"
      "ds_read_b128 v[104:107], v200 offset:17408\n\t"
      "ds_read_b64_tr_b16 a[72:73], v63 offset:18432\n\t"
      "ds_read_b64_tr_b16 a[74:75], v63 offset:22528\n\t"
      "buffer_load_dwordx4 v30, s[8:11], s55 offen offset:3072 sc1 lds\n\t"
      "s_waitcnt lgkmcnt(14)\n\t"
      "v_mfma_f32_32x32x16_bf16 v[64:79], a[140:143], v[120:123], v[64:79]\n\t"
      "ds_read_b128 v[108:111], v200 offset:21504\n\t"
      "ds_read_b64_tr_b16 a[76:77], v63 offset:26624\n\t"
      "ds_read_b64_tr_b16 a[78:79], v63 offset:30720\n\t"
      "s_waitcnt lgkmcnt(14)\n\t"
      "v_mfma_f32_32x32x16_bf16 v[80:95], a[140:143], v[124:127], v[80:95]\n\t"
      "ds_read_b128 v[112:115], v200 offset:18432\n\t"
      "ds_read_b64_tr_b16 a[80:81], v63 offset:32768\n\t"
      "ds_read_b64_tr_b16 a[82:83], v63 offset:36864\n\t"
      "s_waitcnt lgkmcnt(14)\n\t"
      "v_mfma_f32_32x32x16_bf16 v[64:79], a[144:147], v[96:99], v[64:79]\n\t"
      "ds_read_b128 v[116:119], v200 offset:22528\n\t"
      "ds_read_b64_tr_b16 a[84:85], v63 offset:40960\n\t"
      "ds_read_b64_tr_b16 a[86:87], v63 offset:45056\n\t"
      "s_waitcnt lgkmcnt(14)\n\t"
      "v_mfma_f32_32x32x16_bf16 v[80:95], a[144:147], v[100:103], v[80:95]\n\t"
      "ds_read_b128 v[120:123], v200 offset:19456\n\t"
      "ds_read_b64_tr_b16 a[88:89], v63 offset:34816\n\t"
      "ds_read_b64_tr_b16 a[90:91], v63 offset:38912\n\t"
      "s_waitcnt lgkmcnt(14)\n\t"
      "v_mfma_f32_32x32x16_bf16 v[64:79], a[148:151], v[104:107], v[64:79]\n\t"
      "s_waitcnt vmcnt(12)\n\t"
      "s_barrier\n\t"
      "ds_read_b128 v[124:127], v200 offset:23552\n\t"
      "ds_read_b64_tr_b16 a[92:93], v63 offset:43008\n\t"
      "ds_read_b64_tr_b16 a[94:95], v63 offset:47104\n\t"
      "s_waitcnt lgkmcnt(14)\n\t"
      "v_mfma_f32_32x32x16_bf16 v[80:95], a[148:151], v[108:111], v[80:95]\n\t"
      "ds_read_b128 v[96:99], v200 offset:32768\n\t"
      "s_waitcnt lgkmcnt(12)\n\t"
      "v_mfma_f32_32x32x16_bf16 v[64:79], a[152:155], v[112:115], v[64:79]\n\t"
      "ds_read_b128 v[100:103], v200 offset:36864\n\t"
      "s_waitcnt lgkmcnt(10)\n\t"
      "v_mfma_f32_32x32x16_bf16 v[80:95], a[152:155], v[116:119], v[80:95]\n\t"
      "ds_read_b128 v[104:107], v200 offset:33792\n\t"
      "s_waitcnt lgkmcnt(8)\n\t"
      "v_mfma_f32_32x32x16_bf16 v[64:79], a[156:159], v[120:123], v[64:79]\n\t"
      "ds_read_b128 v[108:111], v200 offset:37888\n\t"
      "s_waitcnt lgkmcnt(6)\n\t"
      "v_mfma_f32_32x32x16_bf16 v[80:95], a[156:159], v[124:127], v[80:95]\n\t"
      "ds_read_b128 v[112:115], v200 offset:34816\n\t"
      "s_waitcnt lgkmcnt(4)\n\t"
      "v_mfma_f32_32x32x16_bf16 v[64:79], a[160:163], v[96:99], v[64:79]\n\t"
      "ds_read_b128 v[116:119], v200 offset:38912\n\t"
      "s_waitcnt lgkmcnt(4)\n\t"
      "v_mfma_f32_32x32x16_bf16 v[80:95], a[160:163], v[100:103], v[80:95]\n\t"
      "ds_read_b128 v[120:123], v200 offset:35840\n\t"
      "s_waitcnt lgkmcnt(4)\n\t"
      "v_mfma_f32_32x32x16_bf16 v[64:79], a[164:167], v[104:107], v[64:79]\n\t"
      "s_waitcnt vmcnt(8)\n\t"
      "s_barrier\n\t"
      "ds_read_b128 v[124:127], v200 offset:39936\n\t"
      "s_add_u32 m0, s52, 0x18800\n\t"
      "s_add_u32 s55, s85, 0x6000\n\t"
      "buffer_load_dwordx4 v30, s[8:11], s55 offen sc1 lds\n\t"
      "s_waitcnt lgkmcnt(4)\n\t"
      "v_mfma_f32_32x32x16_bf16 v[80:95], a[164:167], v[108:111], v[80:95]\n\t"
      "ds_read_b128 v[96:99], v200 offset:49152\n\t"
      "s_waitcnt lgkmcnt(4)\n\t"
      "v_mfma_f32_32x32x16_bf16 v[64:79], a[168:171], v[112:115], v[64:79]\n\t"
      "ds_read_b128 v[100:103], v200 offset:53248\n\t"
      "buffer_load_dwordx4 v30, s[8:11], s55 offen offset:1024 sc1 lds\n\t"
      "s_waitcnt lgkmcnt(4)\n\t"
      "v_mfma_f32_32x32x16_bf16 v[80:95], a[168:171], v[116:119], v[80:95]\n\t"
      "ds_read_b128 v[104:107], v200 offset:50176\n\t"
      "s_waitcnt lgkmcnt(4)\n\t"
      "v_mfma_f32_32x32x16_bf16 v[64:79], a[172:175], v[120:123], v[64:79]\n\t"
      "ds_read_b128 v[108:111], v200 offset:54272\n\t"
      "buffer_load_dwordx4 v30, s[8:11], s55 offen offset:2048 sc1 lds\n\t"
      "s_waitcnt lgkmcnt(4)\n\t"
      "v_mfma_f32_32x32x16_bf16 v[80:95], a[172:175], v[124:127], v[80:95]\n\t"
      "ds_read_b128 v[112:115], v200 offset:51200\n\t"
      "s_waitcnt lgkmcnt(4)\n\t"
      "v_mfma_f32_32x32x16_bf16 v[64:79], a[176:179], v[96:99], v[64:79]\n\t"
      "ds_read_b128 v[116:119], v200 offset:55296\n\t"
      "buffer_load_dwordx4 v30, s[8:11], s55 offen offset:3072 sc1 lds\n\t"
      "s_add_u32 m0, s52, 0x1c800\n\t"
      "s_add_u32 s55, s85, 0x7000\n\t"
      "s_waitcnt lgkmcnt(4)\n\t"
      "v_mfma_f32_32x32x16_bf16 v[80:95], a[176:179], v[100:103], v[80:95]\n\t"
      "ds_read_b128 v[120:123], v200 offset:52224\n\t"
      "s_waitcnt lgkmcnt(4)\n\t"
      "v_mfma_f32_32x32x16_bf16 v[64:79], a[180:183], v[104:107], v[64:79]\n\t"
      "s_waitcnt vmcnt(8)\n\t"
      "s_barrier\n\t"
      "ds_read_b128 v[124:127], v200 offset:56320\n\t"
      "buffer_load_dwordx4 v30, s[8:11], s55 offen sc1 lds\n\t"
      "s_waitcnt lgkmcnt(4)\n\t"
      "v_mfma_f32_32x32x16_bf16 v[80:95], a[180:183], v[108:111], v[80:95]\n\t"
      "ds_read_b128 v[96:99], v201 offset:0\n\t"
      "s_waitcnt lgkmcnt(4)\n\t"
      "v_mfma_f32_32x32x16_bf16 v[64:79], a[184:187], v[112:115], v[64:79]\n\t"
      "ds_read_b128 v[100:103], v201 offset:4096\n\t"
      "buffer_load_dwordx4 v30, s[8:11], s55 offen offset:1024 sc1 lds\n\t"
      "s_waitcnt lgkmcnt(4)\n\t"
      "v_mfma_f32_32x32x16_bf16 v[80:95], a[184:187], v[116:119], v[80:95]\n\t"
      "ds_read_b128 v[104:107], v201 offset:1024\n\t"
      "s_waitcnt lgkmcnt(4)\n\t"
      "v_mfma_f32_32x32x16_bf16 v[64:79], a[188:191], v[120:123], v[64:79]\n\t"
      "ds_read_b128 v[108:111], v201 offset:5120\n\t"
      "buffer_load_dwordx4 v30, s[8:11], s55 offen offset:2048 sc1 lds\n\t"
      "s_waitcnt lgkmcnt(4)\n\t"
      "v_mfma_f32_32x32x16_bf16 v[80:95], a[188:191], v[124:127], v[80:95]\n\t"
      "ds_read_b128 v[112:115], v201 offset:2048\n\t"
      "s_waitcnt lgkmcnt(4)\n\t"
      "v_mfma_f32_32x32x16_bf16 v[64:79], a[192:195], v[96:99], v[64:79]\n\t"
      "ds_read_b128 v[116:119], v201 offset:6144\n\t"
      "buffer_load_dwordx4 v30, s[8:11], s55 offen offset:3072 sc1 lds\n\t"
      "s_add_u32 m0, s52, 0x20800\n\t"
      "s_add_u32 s55, s85, 0x8000\n\t"
      "s_waitcnt lgkmcnt(4)\n\t"
      "v_mfma_f32_32x32x16_bf16 v[80:95], a[192:195], v[100:103], v[80:95]\n\t"
      "ds_read_b128 v[120:123], v201 offset:3072\n\t"
      "s_waitcnt lgkmcnt(4)\n\t"
      "v_mfma_f32_32x32x16_bf16 v[64:79], a[196:199], v[104:107], v[64:79]\n\t"
      "s_waitcnt vmcnt(8)\n\t"
      "s_barrier\n\t"
      "ds_read_b128 v[124:127], v201 offset:7168\n\t"
      "buffer_load_dwordx4 v30, s[8:11], s55 offen sc1 lds\n\t"
      "s_waitcnt lgkmcnt(4)\n\t"
      "v_mfma_f32_32x32x16_bf16 v[80:95], a[196:199], v[108:111], v[80:95]\n\t"
      "ds_read_b128 v[96:99], v201 offset:16384\n\t"
      "s_waitcnt lgkmcnt(4)\n\t"
      "v_mfma_f32_32x32x16_bf16 v[64:79], a[200:203], v[112:115], v[64:79]\n\t"
      "ds_read_b128 v[100:103], v201 offset:20480\n\t"
      "buffer_load_dwordx4 v30, s[8:11], s55 offen offset:1024 sc1 lds\n\t"
      "s_waitcnt lgkmcnt(4)\n\t"
      "v_mfma_f32_32x32x16_bf16 v[80:95], a[200:203], v[116:119], v[80:95]\n\t"
      "ds_read_b128 v[104:107], v201 offset:17408\n\t"
      "s_waitcnt lgkmcnt(4)\n\t"
      "v_mfma_f32_32x32x16_bf16 v[64:79], a[204:207], v[120:123], v[64:79]\n\t"
      "ds_read_b128 v[108:111], v201 offset:21504\n\t"
      "buffer_load_dwordx4 v30, s[8:11], s55 offen offset:2048 sc1 lds\n\t"
      "s_waitcnt lgkmcnt(4)\n\t"
      "v_mfma_f32_32x32x16_bf16 v[80:95], a[204:207], v[124:127], v[80:95]\n\t"
      "ds_read_b128 v[112:115], v201 offset:18432\n\t"
      "s_waitcnt lgkmcnt(4)\n\t"
      "v_mfma_f32_32x32x16_bf16 v[64:79], a[208:211], v[96:99], v[64:79]\n\t"
      "ds_read_b128 v[116:119], v201 offset:22528\n\t"
      "buffer_load_dwordx4 v30, s[8:11], s55 offen offset:3072 sc1 lds\n\t"
      "s_add_u32 m0, s52, 0x800\n\t"
      "s_add_u32 s55, s85, 0x9000\n\t"
      "s_waitcnt lgkmcnt(4)\n\t"
      "v_mfma_f32_32x32x16_bf16 v[80:95], a[208:211], v[100:103], v[80:95]\n\t"
      "ds_read_b128 v[120:123], v201 offset:19456\n\t"
      "s_waitcnt lgkmcnt(4)\n\t"
      "v_mfma_f32_32x32x16_bf16 v[64:79], a[212:215], v[104:107], v[64:79]\n\t"
      "s_waitcnt vmcnt(8)\n\t"
      "s_barrier\n\t"
      "ds_read_b128 v[124:127], v201 offset:23552\n\t"
      "buffer_load_dwordx4 v30, s[8:11], s55 offen sc1 lds\n\t"
      "s_waitcnt lgkmcnt(4)\n\t"
      "v_mfma_f32_32x32x16_bf16 v[80:95], a[212:215], v[108:111], v[80:95]\n\t"
      "ds_read_b128 v[96:99], v201 offset:32768\n\t"
      "s_waitcnt lgkmcnt(4)\n\t"
      "v_mfma_f32_32x32x16_bf16 v[64:79], a[216:219], v[112:115], v[64:79]\n\t"
      "ds_read_b128 v[100:103], v201 offset:36864\n\t"
      "buffer_load_dwordx4 v30, s[8:11], s55 offen offset:1024 sc1 lds\n\t"
      "s_waitcnt lgkmcnt(4)\n\t"
      "v_mfma_f32_32x32x16_bf16 v[80:95], a[216:219], v[116:119], v[80:95]\n\t"
      "ds_read_b128 v[104:107], v201 offset:33792\n\t"
      "s_waitcnt lgkmcnt(4)\n\t"
      "v_mfma_f32_32x32x16_bf16 v[64:79], a[220:223], v[120:123], v[64:79]\n\t"
      "ds_read_b128 v[108:111], v201 offset:37888\n\t"
      "buffer_load_dwordx4 v30, s[8:11], s55 offen offset:2048 sc1 lds\n\t"
      "s_waitcnt lgkmcnt(4)\n\t"
      "v_mfma_f32_32x32x16_bf16 v[80:95], a[220:223], v[124:127], v[80:95]\n\t"
      "ds_read_b128 v[112:115], v201 offset:34816\n\t"
      "s_waitcnt lgkmcnt(4)\n\t"
      "v_mfma_f32_32x32x16_bf16 v[64:79], a[224:227], v[96:99], v[64:79]\n\t"
      "ds_read_b128 v[116:119], v201 offset:38912\n\t"
      "buffer_load_dwordx4 v30, s[8:11], s55 offen offset:3072 sc1 lds\n\t"
      "s_add_u32 m0, s52, 0x4800\n\t"
      "s_add_u32 s55, s85, 0xa000\n\t"
      "s_waitcnt lgkmcnt(4)\n\t"
      "v_mfma_f32_32x32x16_bf16 v[80:95], a[224:227], v[100:103], v[80:95]\n\t"
      "ds_read_b128 v[120:123], v201 offset:35840\n\t"
      "s_waitcnt lgkmcnt(4)\n\t"
      "v_mfma_f32_32x32x16_bf16 v[64:79], a[228:231], v[104:107], v[64:79]\n\t"
      "s_waitcnt vmcnt(8)\n\t"
      "s_barrier\n\t"
      "ds_read_b128 v[124:127], v201 offset:39936\n\t"
      "buffer_load_dwordx4 v30, s[8:11], s55 offen sc1 lds\n\t"
      "s_waitcnt lgkmcnt(4)\n\t"
      "v_mfma_f32_32x32x16_bf16 v[80:95], a[228:231], v[108:111], v[80:95]\n\t"
      "ds_read_b128 v[96:99], v201 offset:49152\n\t"
      "s_waitcnt lgkmcnt(4)\n\t"
      "v_mfma_f32_32x32x16_bf16 v[64:79], a[232:235], v[112:115], v[64:79]\n\t"
      "ds_read_b128 v[100:103], v201 offset:53248\n\t"
      "buffer_load_dwordx4 v30, s[8:11], s55 offen offset:1024 sc1 lds\n\t"
      "s_waitcnt lgkmcnt(4)\n\t"
      "v_mfma_f32_32x32x16_bf16 v[80:95], a[232:235], v[116:119], v[80:95]\n\t"
      "ds_read_b128 v[104:107], v201 offset:50176\n\t"
      "s_waitcnt lgkmcnt(4)\n\t"
      "v_mfma_f32_32x32x16_bf16 v[64:79], a[236:239], v[120:123], v[64:79]\n\t"
      "ds_read_b128 v[108:111], v201 offset:54272\n\t"
      "buffer_load_dwordx4 v30, s[8:11], s55 offen offset:2048 sc1 lds\n\t"
      "s_waitcnt lgkmcnt(4)\n\t"
      "v_mfma_f32_32x32x16_bf16 v[80:95], a[236:239], v[124:127], v[80:95]\n\t"
      "ds_read_b128 v[112:115], v201 offset:51200\n\t"
      "s_waitcnt lgkmcnt(4)\n\t"
      "v_mfma_f32_32x32x16_bf16 v[64:79], a[240:243], v[96:99], v[64:79]\n\t"
      "ds_read_b128 v[116:119], v201 offset:55296\n\t"
      "buffer_load_dwordx4 v30, s[8:11], s55 offen offset:3072 sc1 lds\n\t"
      "s_add_u32 m0, s52, 0x8800\n\t"
      "s_add_u32 s55, s85, 0xb000\n\t"
      "s_waitcnt lgkmcnt(4)\n\t"
      "v_mfma_f32_32x32x16_bf16 v[80:95], a[240:243], v[100:103], v[80:95]\n\t"
      "ds_read_b128 v[120:123], v201 offset:52224\n\t"
      "s_waitcnt lgkmcnt(4)\n\t"
      "v_mfma_f32_32x32x16_bf16 v[64:79], a[244:247], v[104:107], v[64:79]\n\t"
      "s_waitcnt vmcnt(8)\n\t"
      "s_barrier\n\t"
      "ds_read_b128 v[124:127], v201 offset:56320\n\t"
      "buffer_load_dwordx4 v30, s[8:11], s55 offen sc1 lds\n\t"
      "s_waitcnt lgkmcnt(4)\n\t"
      "v_mfma_f32_32x32x16_bf16 v[80:95], a[244:247], v[108:111], v[80:95]\n\t"
      "ds_read_b128 v[96:99], v202 offset:0\n\t"
      "s_waitcnt lgkmcnt(4)\n\t"
      "v_mfma_f32_32x32x16_bf16 v[64:79], a[248:251], v[112:115], v[64:79]\n\t"
      "ds_read_b128 v[100:103], v202 offset:4096\n\t"
      "buffer_load_dwordx4 v30, s[8:11], s55 offen offset:1024 sc1 lds\n\t"
      "s_waitcnt lgkmcnt(4)\n\t"
      "v_mfma_f32_32x32x16_bf16 v[80:95], a[248:251], v[116:119], v[80:95]\n\t"
      "ds_read_b128 v[104:107], v202 offset:1024\n\t"
      "s_waitcnt lgkmcnt(4)\n\t"
      "v_mfma_f32_32x32x16_bf16 v[64:79], a[252:255], v[120:123], v[64:79]\n\t"
      "ds_read_b128 v[108:111], v202 offset:5120\n\t"
      "buffer_load_dwordx4 v30, s[8:11], s55 offen offset:2048 sc1 lds\n\t"
      "s_waitcnt lgkmcnt(4)\n\t"
      "v_mfma_f32_32x32x16_bf16 v[80:95], a[252:255], v[124:127], v[80:95]\n\t"
      "ds_read_b128 v[112:115], v202 offset:2048\n\t"
      "s_waitcnt lgkmcnt(4)\n\t"
      "v_mfma_f32_32x32x16_bf16 v[64:79], a[0:3], v[96:99], v[64:79]\n\t"
      "ds_read_b128 v[116:119], v202 offset:6144\n\t"
      "buffer_load_dwordx4 v30, s[8:11], s55 offen offset:3072 sc1 lds\n\t"
      "s_add_u32 m0, s52, 0xc800\n\t"
      "s_add_u32 s55, s85, 0xc000\n\t"
      "s_waitcnt lgkmcnt(4)\n\t"
      "v_mfma_f32_32x32x16_bf16 v[80:95], a[0:3], v[100:103], v[80:95]\n\t"
      "ds_read_b128 v[120:123], v202 offset:3072\n\t"
      "s_waitcnt lgkmcnt(4)\n\t"
      "v_mfma_f32_32x32x16_bf16 v[64:79], a[4:7], v[104:107], v[64:79]\n\t"
      "s_waitcnt vmcnt(8)\n\t"
      "s_barrier\n\t"
      "ds_read_b128 v[124:127], v202 offset:7168\n\t"
      "buffer_load_dwordx4 v30, s[8:11], s55 offen sc1 lds\n\t"
      "s_waitcnt lgkmcnt(4)\n\t"
      "v_mfma_f32_32x32x16_bf16 v[80:95], a[4:7], v[108:111], v[80:95]\n\t"
      "ds_read_b128 v[96:99], v200 offset:0\n\t"
      "s_waitcnt lgkmcnt(4)\n\t"
      "v_mfma_f32_32x32x16_bf16 v[64:79], a[8:11], v[112:115], v[64:79]\n\t"
      "ds_read_b128 v[100:103], v200 offset:4096\n\t"
      "buffer_load_dwordx4 v30, s[8:11], s55 offen offset:1024 sc1 lds\n\t"
      "s_waitcnt lgkmcnt(4)\n\t"
      "v_mfma_f32_32x32x16_bf16 v[80:95], a[8:11], v[116:119], v[80:95]\n\t"
      "ds_read_b128 v[104:107], v200 offset:1024\n\t"
      "s_waitcnt lgkmcnt(4)\n\t"
      "v_mfma_f32_32x32x16_bf16 v[64:79], a[12:15], v[120:123], v[64:79]\n\t"
      "ds_read_b128 v[108:111], v200 offset:5120\n\t"
      "buffer_load_dwordx4 v30, s[8:11], s55 offen offset:2048 sc1 lds\n\t"
      "s_waitcnt lgkmcnt(4)\n\t"
      "v_mfma_f32_32x32x16_bf16 v[80:95], a[12:15], v[124:127], v[80:95]\n\t"
      "ds_read_b128 v[112:115], v200 offset:2048\n\t"
      "s_waitcnt lgkmcnt(4)\n\t"
      "v_mfma_f32_32x32x16_bf16 v[64:79], a[16:19], v[96:99], v[64:79]\n\t"
      "ds_read_b128 v[116:119], v200 offset:6144\n\t"
      "buffer_load_dwordx4 v30, s[8:11], s55 offen offset:3072 sc1 lds\n\t"
      "s_add_u32 m0, s52, 0x10800\n\t"
      "s_add_u32 s55, s85, 0xd000\n\t"
      "s_waitcnt lgkmcnt(4)\n\t"
      "v_mfma_f32_32x32x16_bf16 v[80:95], a[16:19], v[100:103], v[80:95]\n\t"
      "ds_read_b128 v[120:123], v200 offset:3072\n\t"
      "s_waitcnt lgkmcnt(4)\n\t"
      "v_mfma_f32_32x32x16_bf16 v[64:79], a[20:23], v[104:107], v[64:79]\n\t"
      "s_waitcnt vmcnt(8)\n\t"
      "s_barrier\n\t"
      "ds_read_b128 v[124:127], v200 offset:7168\n\t"
      "buffer_load_dwordx4 v30, s[8:11], s55 offen sc1 lds\n\t"
      "s_waitcnt lgkmcnt(4)\n\t"
      "v_mfma_f32_32x32x16_bf16 v[80:95], a[20:23], v[108:111], v[80:95]\n\t"
      "ds_read_b128 v[96:99], v200 offset:16384\n\t"
      "s_waitcnt lgkmcnt(4)\n\t"
      "v_mfma_f32_32x32x16_bf16 v[64:79], a[24:27], v[112:115], v[64:79]\n\t"
      "ds_read_b128 v[100:103], v200 offset:20480\n\t"
      "buffer_load_dwordx4 v30, s[8:11], s55 offen offset:1024 sc1 lds\n\t"
      "s_waitcnt lgkmcnt(4)\n\t"
      "v_mfma_f32_32x32x16_bf16 v[80:95], a[24:27], v[116:119], v[80:95]\n\t"
      "ds_read_b128 v[104:107], v200 offset:17408\n\t"
      "s_waitcnt lgkmcnt(4)\n\t"
      "v_mfma_f32_32x32x16_bf16 v[64:79], a[28:31], v[120:123], v[64:79]\n\t"
      "ds_read_b128 v[108:111], v200 offset:21504\n\t"
      "buffer_load_dwordx4 v30, s[8:11], s55 offen offset:2048 sc1 lds\n\t"
      "s_waitcnt lgkmcnt(4)\n\t"
      "v_mfma_f32_32x32x16_bf16 v[80:95], a[28:31], v[124:127], v[80:95]\n\t"
      "ds_read_b128 v[112:115], v200 offset:18432\n\t"
      "s_waitcnt lgkmcnt(4)\n\t"
      "v_mfma_f32_32x32x16_bf16 v[64:79], a[32:35], v[96:99], v[64:79]\n\t"
      "ds_read_b128 v[116:119], v200 offset:22528\n\t"
      "buffer_load_dwordx4 v30, s[8:11], s55 offen offset:3072 sc1 lds\n\t"
      "s_waitcnt lgkmcnt(4)\n\t"
      "v_mfma_f32_32x32x16_bf16 v[80:95], a[32:35], v[100:103], v[80:95]\n\t"
      "ds_read_b128 v[120:123], v200 offset:19456\n\t"
      "s_waitcnt lgkmcnt(4)\n\t"
      "v_mfma_f32_32x32x16_bf16 v[64:79], a[36:39], v[104:107], v[64:79]\n\t"
      "s_waitcnt vmcnt(8)\n\t"
      "s_barrier\n\t"
      "ds_read_b128 v[124:127], v200 offset:23552\n\t"
      "s_waitcnt lgkmcnt(4)\n\t"
      "v_mfma_f32_32x32x16_bf16 v[80:95], a[36:39], v[108:111], v[80:95]\n\t"
      "ds_read_b128 v[96:99], v200 offset:32768\n\t"
      "s_waitcnt lgkmcnt(4)\n\t"
      "v_mfma_f32_32x32x16_bf16 v[64:79], a[40:43], v[112:115], v[64:79]\n\t"
      "ds_read_b128 v[100:103], v200 offset:36864\n\t"
      "s_waitcnt lgkmcnt(4)\n\t"
      "v_mfma_f32_32x32x16_bf16 v[80:95], a[40:43], v[116:119], v[80:95]\n\t"
      "ds_read_b128 v[104:107], v200 offset:33792\n\t"
      "s_waitcnt lgkmcnt(4)\n\t"
      "v_mfma_f32_32x32x16_bf16 v[64:79], a[44:47], v[120:123], v[64:79]\n\t"
      "ds_read_b128 v[108:111], v200 offset:37888\n\t"
      "s_waitcnt lgkmcnt(4)\n\t"
      "v_mfma_f32_32x32x16_bf16 v[80:95], a[44:47], v[124:127], v[80:95]\n\t"
      "ds_read_b128 v[112:115], v200 offset:34816\n\t"
      "s_waitcnt lgkmcnt(4)\n\t"
      "v_mfma_f32_32x32x16_bf16 v[64:79], a[48:51], v[96:99], v[64:79]\n\t"
      "ds_read_b128 v[116:119], v200 offset:38912\n\t"
      "s_waitcnt lgkmcnt(4)\n\t"
      "v_mfma_f32_32x32x16_bf16 v[80:95], a[48:51], v[100:103], v[80:95]\n\t"
      "ds_read_b128 v[120:123], v200 offset:35840\n\t"
      "s_waitcnt lgkmcnt(4)\n\t"
      "v_mfma_f32_32x32x16_bf16 v[64:79], a[52:55], v[104:107], v[64:79]\n\t"
      "s_waitcnt vmcnt(4)\n\t"
      "s_barrier\n\t"
      "ds_read_b128 v[124:127], v200 offset:39936\n\t"
      "s_waitcnt lgkmcnt(4)\n\t"
      "v_mfma_f32_32x32x16_bf16 v[80:95], a[52:55], v[108:111], v[80:95]\n\t"
      "ds_read_b128 v[96:99], v200 offset:49152\n\t"
      "s_waitcnt lgkmcnt(4)\n\t"
      "v_mfma_f32_32x32x16_bf16 v[64:79], a[56:59], v[112:115], v[64:79]\n\t"
      "ds_read_b128 v[100:103], v200 offset:53248\n\t"
      "s_waitcnt lgkmcnt(4)\n\t"
      "v_mfma_f32_32x32x16_bf16 v[80:95], a[56:59], v[116:119], v[80:95]\n\t"
      "ds_read_b128 v[104:107], v200 offset:50176\n\t"
      "s_waitcnt lgkmcnt(4)\n\t"
      "v_mfma_f32_32x32x16_bf16 v[64:79], a[60:63], v[120:123], v[64:79]\n\t"
      "ds_read_b128 v[108:111], v200 offset:54272\n\t"
      "s_waitcnt lgkmcnt(4)\n\t"
      "v_mfma_f32_32x32x16_bf16 v[80:95], a[60:63], v[124:127], v[80:95]\n\t"
      "ds_read_b128 v[112:115], v200 offset:51200\n\t"
      "s_waitcnt lgkmcnt(4)\n\t"
      "v_mfma_f32_32x32x16_bf16 v[64:79], a[64:67], v[96:99], v[64:79]\n\t"
      "ds_read_b128 v[116:119], v200 offset:55296\n\t"
      "s_waitcnt lgkmcnt(4)\n\t"
      "v_mfma_f32_32x32x16_bf16 v[80:95], a[64:67], v[100:103], v[80:95]\n\t"
      "ds_read_b128 v[120:123], v200 offset:52224\n\t"
      "s_waitcnt lgkmcnt(4)\n\t"
      "v_mfma_f32_32x32x16_bf16 v[64:79], a[68:71], v[104:107], v[64:79]\n\t"
      "s_waitcnt vmcnt(0)\n\t"
      "s_barrier\n\t"
      "ds_read_b128 v[124:127], v200 offset:56320\n\t"
      "s_waitcnt lgkmcnt(4)\n\t"
      "v_mfma_f32_32x32x16_bf16 v[80:95], a[68:71], v[108:111], v[80:95]\n\t"
      "ds_read_b128 v[96:99], v201 offset:0\n\t"
      "s_waitcnt lgkmcnt(4)\n\t"
      "v_mfma_f32_32x32x16_bf16 v[64:79], a[72:75], v[112:115], v[64:79]\n\t"
      "ds_read_b128 v[100:103], v201 offset:4096\n\t"
      "s_waitcnt lgkmcnt(4)\n\t"
      "v_mfma_f32_32x32x16_bf16 v[80:95], a[72:75], v[116:119], v[80:95]\n\t"
      "ds_read_b128 v[104:107], v201 offset:1024\n\t"
      "s_waitcnt lgkmcnt(4)\n\t"
      "v_mfma_f32_32x32x16_bf16 v[64:79], a[76:79], v[120:123], v[64:79]\n\t"
      "ds_read_b128 v[108:111], v201 offset:5120\n\t"
      "s_waitcnt lgkmcnt(4)\n\t"
      "v_mfma_f32_32x32x16_bf16 v[80:95], a[76:79], v[124:127], v[80:95]\n\t"
      "ds_read_b128 v[112:115], v201 offset:2048\n\t"
      "s_waitcnt lgkmcnt(4)\n\t"
      "v_mfma_f32_32x32x16_bf16 v[64:79], a[80:83], v[96:99], v[64:79]\n\t"
      "ds_read_b128 v[116:119], v201 offset:6144\n\t"
      "s_waitcnt lgkmcnt(4)\n\t"
      "v_mfma_f32_32x32x16_bf16 v[80:95], a[80:83], v[100:103], v[80:95]\n\t"
      "ds_read_b128 v[120:123], v201 offset:3072\n\t"
      "s_waitcnt lgkmcnt(4)\n\t"
      "v_mfma_f32_32x32x16_bf16 v[64:79], a[84:87], v[104:107], v[64:79]\n\t"
      "ds_read_b128 v[124:127], v201 offset:7168\n\t"
      "s_waitcnt lgkmcnt(4)\n\t"
      "v_mfma_f32_32x32x16_bf16 v[80:95], a[84:87], v[108:111], v[80:95]\n\t"
      "s_waitcnt lgkmcnt(3)\n\t"
      "v_mfma_f32_32x32x16_bf16 v[64:79], a[88:91], v[112:115], v[64:79]\n\t"
      "s_waitcnt lgkmcnt(2)\n\t"
      "v_mfma_f32_32x32x16_bf16 v[80:95], a[88:91], v[116:119], v[80:95]\n\t"
      "s_waitcnt lgkmcnt(1)\n\t"
      "v_mfma_f32_32x32x16_bf16 v[64:79], a[92:95], v[120:123], v[64:79]\n\t"
      "s_waitcnt lgkmcnt(0)\n\t"
      "v_mfma_f32_32x32x16_bf16 v[80:95], a[92:95], v[124:127], v[80:95]\n\t"
      "s_cmp_eq_u32 s46, 0\n\t"
      "s_cbranch_scc1 Lp4a%=\n\t"
      "s_nop 7\n\t"
      "ds_write_b128 v31, v[64:67] offset:18432\n\t"
      "ds_write_b128 v31, v[68:71] offset:19456\n\t"
      "ds_write_b128 v31, v[72:75] offset:20480\n\t"
      "ds_write_b128 v31, v[76:79] offset:21504\n\t"
      "s_branch Lp4b%=\n\t"
      "Lp4a%=:\n\t"
      "s_nop 9\n\t"
      "ds_write_b128 v31, v[80:83] offset:18432\n\t"
      "ds_write_b128 v31, v[84:87] offset:19456\n\t"
      "ds_write_b128 v31, v[88:91] offset:20480\n\t"
      "ds_write_b128 v31, v[92:95] offset:21504\n\t"
      "v_mov_b64 v[80:81], v[64:65]\n\t"
      "v_mov_b64 v[82:83], v[66:67]\n\t"
      "v_mov_b64 v[84:85], v[68:69]\n\t"
      "v_mov_b64 v[86:87], v[70:71]\n\t"
      "v_mov_b64 v[88:89], v[72:73]\n\t"
      "v_mov_b64 v[90:91], v[74:75]\n\t"
      "v_mov_b64 v[92:93], v[76:77]\n\t"
      "v_mov_b64 v[94:95], v[78:79]\n\t"
      "Lp4b%=:\n\t"
      "s_waitcnt lgkmcnt(0)\n\t"
      "s_barrier\n\t"
      "ds_read_b128 v[96:99], v33 offset:18432\n\t"
      "ds_read_b128 v[100:103], v33 offset:19456\n\t"
      "ds_read_b128 v[104:107], v33 offset:20480\n\t"
      "ds_read_b128 v[108:111], v33 offset:21504\n\t"
      "s_waitcnt lgkmcnt(3)\n\t"
      "v_pk_add_f32 v[96:97], v[96:97], v[80:81]\n\t"
      "v_pk_add_f32 v[98:99], v[98:99], v[82:83]\n\t"
      "v_fma_f32 v96, v96, v51, v140\n\t"
      "v_fma_f32 v97, v97, v51, v141\n\t"
      "v_fma_f32 v98, v98, v51, v142\n\t"
      "v_fma_f32 v99, v99, v51, v143\n\t"
      "v_cvt_pk_bf16_f32 v112, v96, v97\n\t"
      "v_cvt_pk_bf16_f32 v113, v98, v99\n\t"
      "s_waitcnt lgkmcnt(2)\n\t"
      "v_pk_add_f32 v[100:101], v[100:101], v[84:85]\n\t"
      "v_pk_add_f32 v[102:103], v[102:103], v[86:87]\n\t"
      "v_fma_f32 v100, v100, v51, v144\n\t"
      "v_fma_f32 v101, v101, v51, v145\n\t"
      "v_fma_f32 v102, v102, v51, v146\n\t"
      "v_fma_f32 v103, v103, v51, v147\n\t"
      "v_cvt_pk_bf16_f32 v114, v100, v101\n\t"
      "v_cvt_pk_bf16_f32 v115, v102, v103\n\t"
      "s_nop 0\n\t"
      "v_permlane32_swap_b32 v112, v114\n\t"
      "v_permlane32_swap_b32 v113, v115\n\t"
      "buffer_store_dwordx4 v[112:115], v45, s[36:39], 0 offen\n\t"
      "s_waitcnt lgkmcnt(1)\n\t"
      "v_pk_add_f32 v[104:105], v[104:105], v[88:89]\n\t"
      "v_pk_add_f32 v[106:107], v[106:107], v[90:91]\n\t"
      "v_fma_f32 v104, v104, v51, v148\n\t"
      "v_fma_f32 v105, v105, v51, v149\n\t"
      "v_fma_f32 v106, v106, v51, v150\n\t"
      "v_fma_f32 v107, v107, v51, v151\n\t"
      "v_cvt_pk_bf16_f32 v116, v104, v105\n\t"
      "v_cvt_pk_bf16_f32 v117, v106, v107\n\t"
      "s_waitcnt lgkmcnt(0)\n\t"
      "v_pk_add_f32 v[108:109], v[108:109], v[92:93]\n\t"
      "v_pk_add_f32 v[110:111], v[110:111], v[94:95]\n\t"
      "v_fma_f32 v108, v108, v51, v152\n\t"
      "v_fma_f32 v109, v109, v51, v153\n\t"
      "v_fma_f32 v110, v110, v51, v154\n\t"
      "v_fma_f32 v111, v111, v51, v155\n\t"
      "v_cvt_pk_bf16_f32 v118, v108, v109\n\t"
      "v_cvt_pk_bf16_f32 v119, v110, v111\n\t"
      "s_nop 0\n\t"
      "v_permlane32_swap_b32 v116, v118\n\t"
      "v_permlane32_swap_b32 v117, v119\n\t"
      "buffer_store_dwordx4 v[116:119], v45, s[36:39], 0 offen offset:32\n\t"
      "s_endpgm\n\t"
      "Lslow1%=:\n\t"
      "s_cmp_lg_u32 s3, 0\n\t"
      "s_cbranch_scc1 Lg1ok%=\n\t"
      "s_mov_b32 s3, 1\n\t"
      "s_mov_b32 s68, 196608\n\t"
      "buffer_load_dword v64, v22, s[8:11], s68 offen sc1\n\t"
      "s_movk_i32 s73, 0x7fff\n\t"
      "s_branch Ls2d_chk%=\n\t"
      "Ls2d_again%=:\n\t"
      "s_sleep 2\n\t"
      "buffer_load_dword v64, v22, s[8:11], s68 offen sc1\n\t"
      "Ls2d_chk%=:\n\t"
      "s_waitcnt vmcnt(0)\n\t"
      "v_cmp_ne_u32 vcc, s53, v64\n\t"
      "s_sub_u32 s73, s73, 1\n\t"
      "s_cselect_b64 s[88:89], 0, vcc\n\t"
      "s_cmp_lg_u64 s[88:89], 0\n\t"
      "s_cbranch_scc1 Ls2d_again%=\n\t"
      "s_waitcnt lgkmcnt(0)\n\t"
      "s_barrier\n\t"
      "s_add_u32 m0, s52, 0x800\n\t"
      "s_add_u32 s55, s54, 0x0\n\t"
      "buffer_load_dwordx4 v34, s[32:35], s55 offen lds\n\t"
      "s_add_u32 s55, s54, 0xdfc00\n\t"
      "buffer_load_dwordx4 v34, s[32:35], s55 offen offset:1024 lds\n\t"
      "s_add_u32 s55, s54, 0x7800\n\t"
      "buffer_load_dwordx4 v34, s[32:35], s55 offen offset:2048 lds\n\t"
      "s_add_u32 s55, s54, 0xe7400\n\t"
      "buffer_load_dwordx4 v34, s[32:35], s55 offen offset:3072 lds\n\t"
      "s_add_u32 m0, s52, 0x4800\n\t"
      "s_add_u32 s55, s54, 0x10000\n\t"
      "buffer_load_dwordx4 v34, s[32:35], s55 offen lds\n\t"
      "s_add_u32 s55, s54, 0xefc00\n\t"
      "buffer_load_dwordx4 v34, s[32:35], s55 offen offset:1024 lds\n\t"
      "s_add_u32 s55, s54, 0x17800\n\t"
      "buffer_load_dwordx4 v34, s[32:35], s55 offen offset:2048 lds\n\t"
      "s_add_u32 s55, s54, 0xf7400\n\t"
      "buffer_load_dwordx4 v34, s[32:35], s55 offen offset:3072 lds\n\t"
      "s_branch Lg1%=\n\t"
      "Lfin%=:\n\t"
      ""
      :
      : "{s[0:1]}"(__builtin_amdgcn_kernarg_segment_ptr()), "{s2}"(blockIdx.x), "{v0}"(threadIdx.x)
      : "v1", "v2", "v3", "v4", "v5", "v6", "v7", "v8", "v9", "v10", "v11", "v12", "v13", "v14", "v15", "v16", "v17", "v18", "v19", "v20", "v21", "v22", "v23", "v24", "v25", "v26", "v27", "v28", "v29", "v30", "v31", "v32", "v33", "v34", "v35", "v36", "v37", "v38", "v39", "v40", "v41", "v42", "v43", "v44", "v45", "v46", "v47", "v48", "v49", "v50", "v51", "v52", "v53", "v54", "v55", "v56", "v57", "v58", "v59", "v60", "v61", "v62", "v63", "v64", "v65", "v66", "v67", "v68", "v69", "v70", "v71", "v72", "v73", "v74", "v75", "v76", "v77", "v78", "v79", "v80", "v81", "v82", "v83", "v84", "v85", "v86", "v87", "v88", "v89", "v90", "v91", "v92", "v93", "v94", "v95", "v96", "v97", "v98", "v99", "v100", "v101", "v102", "v103", "v104", "v105", "v106", "v107", "v108", "v109", "v110", "v111", "v112", "v113", "v114", "v115", "v116", "v117", "v118", "v119", "v120", "v121", "v122", "v123", "v124", "v125", "v126", "v127", "v128", "v129", "v130", "v131", "v132", "v133", "v134", "v135", "v136", "v137", "v138", "v139", "v140", "v141", "v142", "v143", "v144", "v145", "v146", "v147", "v148", "v149", "v150", "v151", "v152", "v153", "v154", "v155", "v156", "v157", "v158", "v159", "v160", "v161", "v162", "v163", "v164", "v165", "v166", "v167", "v168", "v169", "v170", "v171", "v172", "v173", "v174", "v175", "v176", "v177", "v178", "v179", "v180", "v181", "v182", "v183", "v184", "v185", "v186", "v187", "v188", "v189", "v190", "v191", "v192", "v193", "v194", "v195", "v196", "v197", "v198", "v199", "v200", "v201", "v202", "v203", "v204", "v205", "v206", "v207", "v208", "v209", "v210", "v211", "v212", "v213", "v214", "v215", "v216", "v217", "v218", "v219", "v220", "v221", "v222", "v223", "v224", "v225", "v226", "v227", "v228", "v229", "v230", "v231", "v232", "v233", "v234", "v235", "v236", "v237", "v238", "v239", "v240", "v241", "v242", "v243", "v244", "v245", "v246", "v247", "v248", "v249", "v250", "v251", "v252", "v253", "v254", "v255", "a0", "a1", "a2", "a3", "a4", "a5", "a6", "a7", "a8", "a9", "a10", "a11", "a12", "a13", "a14", "a15", "a16", "a17", "a18", "a19", "a20", "a21", "a22", "a23", "a24", "a25", "a26", "a27", "a28", "a29", "a30", "a31", "a32", "a33", "a34", "a35", "a36", "a37", "a38", "a39", "a40", "a41", "a42", "a43", "a44", "a45", "a46", "a47", "a48", "a49", "a50", "a51", "a52", "a53", "a54", "a55", "a56", "a57", "a58", "a59", "a60", "a61", "a62", "a63", "a64", "a65", "a66", "a67", "a68", "a69", "a70", "a71", "a72", "a73", "a74", "a75", "a76", "a77", "a78", "a79", "a80", "a81", "a82", "a83", "a84", "a85", "a86", "a87", "a88", "a89", "a90", "a91", "a92", "a93", "a94", "a95", "a96", "a97", "a98", "a99", "a100", "a101", "a102", "a103", "a104", "a105", "a106", "a107", "a108", "a109", "a110", "a111", "a112", "a113", "a114", "a115", "a116", "a117", "a118", "a119", "a120", "a121", "a122", "a123", "a124", "a125", "a126", "a127", "a128", "a129", "a130", "a131", "a132", "a133", "a134", "a135", "a136", "a137", "a138", "a139", "a140", "a141", "a142", "a143", "a144", "a145", "a146", "a147", "a148", "a149", "a150", "a151", "a152", "a153", "a154", "a155", "a156", "a157", "a158", "a159", "a160", "a161", "a162", "a163", "a164", "a165", "a166", "a167", "a168", "a169", "a170", "a171", "a172", "a173", "a174", "a175", "a176", "a177", "a178", "a179", "a180", "a181", "a182", "a183", "a184", "a185", "a186", "a187", "a188", "a189", "a190", "a191", "a192", "a193", "a194", "a195", "a196", "a197", "a198", "a199", "a200", "a201", "a202", "a203", "a204", "a205", "a206", "a207", "a208", "a209", "a210", "a211", "a212", "a213", "a214", "a215", "a216", "a217", "a218", "a219", "a220", "a221", "a222", "a223", "a224", "a225", "a226", "a227", "a228", "a229", "a230", "a231", "a232", "a233", "a234", "a235", "a236", "a237", "a238", "a239", "a240", "a241", "a242", "a243", "a244", "a245", "a246", "a247", "a248", "a249", "a250", "a251", "a252", "a253", "a254", "a255", "s3", "s4", "s5", "s6", "s7", "s8", "s9", "s10", "s11", "s12", "s13", "s14", "s15", "s16", "s17", "s18", "s19", "s20", "s21", "s22", "s23", "s24", "s25", "s26", "s27", "s28", "s29", "s30", "s31", "s33", "s34", "s35", "s36", "s37", "s38", "s39", "s40", "s41", "s42", "s43", "s44", "s45", "s46", "s47", "s48", "s49", "s50", "s51", "s52", "s53", "s54", "s55", "s56", "s57", "s58", "s59", "s60", "s61", "s62", "s63", "s64", "s65", "s66", "s67", "s68", "s69", "s70", "s71", "s72", "s73", "s74", "s75", "s76", "s77", "s78", "s79", "s80", "s81", "s82", "s83", "s84", "s85", "s86", "s87", "s88", "s89", "s90", "s91", "s92", "s93", "s94", "s95", "s96", "s97", "s98", "s99", "vcc", "scc", "memory");
}

extern "C" void kernel_launch(void* const* d_in, const int* in_sizes, int n_in, void* d_out, int out_size, void* d_ws, size_t ws_size,
                              hipStream_t stream) {
  (void)hipFuncSetAttribute((const void*)fused_block, hipFuncAttributeMaxDynamicSharedMemorySize, LDS_BYTES);
  fused_block<<<256, 256, LDS_BYTES, stream>>>((const u16*)d_in[0], (const u16*)d_in[1], (const u16*)d_in[2], (const u16*)d_in[3],
                                               (const u16*)d_in[4], (const u16*)d_in[5], (const u16*)d_in[6], (const u16*)d_in[8],
                                               (const u16*)d_in[9], (u16*)d_out, (char*)d_ws);
}
```
